```python
import torch

def _softmax(x, axis=-1):
    return torch.softmax(x, dim=axis)
def _gelu(x):
    return torch.nn.functional.gelu(x, approximate="tanh")
def _silu(x):
    return torch.nn.functional.silu(x)
def _sigmoid(x):
    return torch.sigmoid(x)
def _layernorm(x, axis=-1, eps=1e-5):
    m = x.mean(dim=axis, keepdim=True)
    v = x.var(dim=axis, keepdim=True, unbiased=False)
    return (x - m) / torch.sqrt(v + eps)
def _scatter_add(tgt, idx, src, axis=0):
    return tgt.clone().index_add_(axis, idx, src)
def _segment_sum(src, seg, n):
    out = torch.zeros((n,) + src.shape[1:], dtype=src.dtype)
    return out.index_add_(0, seg, src)
def _pad(x, pw):
    flat = tuple(v for pair in reversed(pw) for v in pair)
    return torch.nn.functional.pad(x, flat)

kernel_name = 'concat_heads__softmax_full_mha__mha_block__prompt'

def setup_inputs():

    _w = (lambda a: (lambda t: t.to(torch.bfloat16) if t.is_floating_point() else t)(torch.as_tensor(a)))
    def _A_setup():
        B, S, D = 1, 2048, 1024
        return {
            "x": _w((lambda *s: torch.randn(*s, dtype=torch.bfloat16) * 0.2)(B, S, D)),
            "wqkv": _w((lambda *s: torch.randn(*s, dtype=torch.bfloat16) * 0.2)(D, 3 * D)),
            "wo": _w((lambda *s: torch.randn(*s, dtype=torch.bfloat16) * 0.05)(D, D)),
        }
    _A_in = _A_setup()

    def _B_setup():
        B, S, D, H = 1, 2048, 1024, 32
        return {
            "x": _w((lambda *s: torch.randn(*s, dtype=torch.bfloat16) * 0.2)(B, S, D)),
            "wqkv": _w((lambda *s: torch.randn(*s, dtype=torch.bfloat16) * 0.2)(D, 3 * D)),
            "wo": _w((lambda *s: torch.randn(*s, dtype=torch.bfloat16) * 0.05)(D, D)),
        }
    _B_in = _B_setup()

    return {
        "A_x": _A_in["x"],
        "A_wqkv": _A_in["wqkv"],
        "A_wo": _A_in["wo"],
        "B_x": _B_in["x"],
        "B_wqkv": _B_in["wqkv"],
        "B_wo": _B_in["wo"],
    }

def reference(A_x, A_wqkv, A_wo, B_x, B_wqkv, B_wo):
    def _A_ref(x, wqkv, wo):
        B, S, D = x.shape
        H = 32; Dh = D // H
        qkv = (lambda x, *d: x.permute(*d))((x @ wqkv).reshape(B, S, 3, H, Dh), 2, 0, 3, 1, 4)
        q, k, v = qkv[0], qkv[1], qkv[2]
        scale = Dh ** -0.5
        scores = torch.einsum("bhqd,bhkd->bhqk", q, k) * scale
        p = _softmax(scores, axis=-1)
        o = torch.einsum("bhqk,bhkd->bhqd", p, v)
        return (lambda x, *d: x.permute(*d))(o, 0, 2, 1, 3).reshape(B, S, D) @ wo

    def _B_ref(x, wqkv, wo):
        B, S, D = x.shape
        H = 32
        Dh = D // H
        qkv = (lambda x, *d: x.permute(*d))((x @ wqkv).reshape(B, S, 3, H, Dh), 2, 0, 3, 1, 4)
        q, k, v = qkv[0], qkv[1], qkv[2]
        scores = torch.einsum("bhqd,bhkd->bhqk", q, k) * (Dh ** -0.5)
        p = _softmax(scores, axis=-1)
        o = torch.einsum("bhqk,bhkd->bhqd", p, v)
        return (lambda x, *d: x.permute(*d))(o, 0, 2, 1, 3).reshape(B, S, D) @ wo

    a = _A_ref(A_x, A_wqkv, A_wo)
    b = _B_ref(B_x, B_wqkv, B_wo)
    return torch.concatenate([a, b], axis=-1)
```

```cpp
#include <hip/hip_runtime.h>
#include <hip/hip_bf16.h>
#include <stdint.h>

typedef float f32x16 __attribute__((ext_vector_type(16)));
typedef float f32x4 __attribute__((ext_vector_type(4)));
typedef float f32x2 __attribute__((ext_vector_type(2)));
typedef short bf16x8 __attribute__((ext_vector_type(8)));
typedef short s16x4 __attribute__((ext_vector_type(4)));
typedef __bf16 bf16x2_t __attribute__((ext_vector_type(2)));
typedef unsigned u32x4 __attribute__((ext_vector_type(4)));
typedef short v4i16_t __attribute__((ext_vector_type(4)));
#define LAS __attribute__((address_space(3)))
#define DI __device__ __forceinline__
#define MFMA32(a, b, c) __builtin_amdgcn_mfma_f32_32x32x16_bf16(a, b, c, 0, 0, 0)
#define MFMA16(a, b, c) __builtin_amdgcn_mfma_f32_16x16x32_bf16(a, b, c, 0, 0, 0)

constexpr int S = 2048, D = 1024, H = 32, DH = 32;
constexpr size_t WS_Q = 0;
constexpr size_t WS_KV = 8u << 20;
constexpr size_t WS_O = 24u << 20;
constexpr size_t WS_KMAX = 32u << 20;
constexpr float QSCALE = 0.17677669529663687f * 1.4426950408889634f;

DI unsigned cvtpk(float lo, float hi) { f32x2 v = {lo, hi}; bf16x2_t b = __builtin_convertvector(v, bf16x2_t); return __builtin_bit_cast(unsigned, b); }
DI u32x4 pack8(const f32x16& a, int s) { u32x4 w; w[0] = cvtpk(a[8*s+0], a[8*s+1]); w[1] = cvtpk(a[8*s+2], a[8*s+3]); w[2] = cvtpk(a[8*s+4], a[8*s+5]); w[3] = cvtpk(a[8*s+6], a[8*s+7]); return w; }
DI float sumsq8(u32x4 w) { float r = 0.f;
#pragma unroll
  for (int i = 0; i < 4; ++i) { float lo = __uint_as_float(w[i] << 16), hi = __uint_as_float(w[i] & 0xffff0000u); r = fmaf(lo, lo, r); r = fmaf(hi, hi, r); } return r; }
DI s16x4 vtr(LAS const char* p) { return __builtin_bit_cast(s16x4, __builtin_amdgcn_ds_read_tr16_b64_v4i16((LAS v4i16_t*)p)); }
DI bf16x8 cat(s16x4 a, s16x4 b) { return (bf16x8){a[0], a[1], a[2], a[3], b[0], b[1], b[2], b[3]}; }
DI void glds16(const void* g, LAS char* l) { __builtin_amdgcn_global_load_lds((const __attribute__((address_space(1))) void*)g, (LAS void*)l, 16, 0, 0); }

constexpr int G1_STAGE = 32768 + 24576;
__global__ __launch_bounds__(512) void qkv_kernel(const uint16_t* __restrict__ xa, const uint16_t* __restrict__ wa, const uint16_t* __restrict__ xb, const uint16_t* __restrict__ wb, char* __restrict__ ws) {
  __shared__ __attribute__((aligned(16))) char smem[2 * G1_STAGE];
  LAS char* lds = (LAS char*)smem;
  const int tid = threadIdx.x, lane = tid & 63, wid = __builtin_amdgcn_readfirstlane(tid >> 6);
  const int xcd = blockIdx.x & 7, j = blockIdx.x >> 3;
  const int b = xcd >> 2, xq = xcd & 3;
  const int rb = (xq >> 1) * 4 + (j & 3), hp = (xq & 1) * 8 + (j >> 2);
  const uint16_t* x = b ? xb : xa; const uint16_t* w = b ? wb : wa;
  const int wm = wid >> 1, wn = wid & 1, hi = lane >> 5, l31 = lane & 31;
  const char* xs[4]; const char* wsrc[3];
#pragma unroll
  for (int i = 0; i < 4; ++i) { int g = 4 * wid + i, row = 8 * g + (lane >> 3), ch = (lane & 7) ^ ((row >> 1) & 7); xs[i] = (const char*)x + (size_t)(rb * 256 + row) * 2048 + ch * 16; }
#pragma unroll
  for (int i = 0; i < 3; ++i) { int p = 3 * wid + i, seg = p >> 3, g = p & 7, k = 8 * g + (lane >> 3), ch = (lane & 7) ^ (((k >> 1) & 1) << 2); wsrc[i] = (const char*)w + (size_t)k * 6144 + (seg * 1024 + 64 * hp) * 2 + ch * 16; }
  auto stage = [&](int buf, int ks) {
    LAS char* base = lds + buf * G1_STAGE;
#pragma unroll
    for (int i = 0; i < 4; ++i) glds16(xs[i] + ks * 128, base + (4 * wid + i) * 1024);
#pragma unroll
    for (int i = 0; i < 3; ++i) glds16(wsrc[i] + (size_t)ks * 64 * 6144, base + 32768 + (3 * wid + i) * 1024);
  };
  int xoff[4];
#pragma unroll
  for (int s = 0; s < 4; ++s) { int row = 64 * wm + l31; xoff[s] = row * 128 + (((2 * s + hi) ^ ((row >> 1) & 7)) * 16); }
  int woff; { int g = lane >> 4, i = lane & 15, q = i >> 2, p = i & 3; int k = 8 * (g >> 1) + q, ch = (4 * wn + 2 * (g & 1) + (p >> 1)) ^ (((k >> 1) & 1) << 2); woff = 32768 + k * 128 + ch * 16 + (p & 1) * 8; }
  f32x16 aq[2] = {}, ak[2] = {}, av[2] = {};
  stage(0, 0);
  for (int ks = 0; ks < 16; ++ks) {
    asm volatile("s_waitcnt vmcnt(0)" ::: "memory");
    __syncthreads();
    if (ks + 1 < 16) stage((ks + 1) & 1, ks + 1);
    LAS char* base = lds + (ks & 1) * G1_STAGE;
#pragma unroll
    for (int s = 0; s < 4; ++s) {
      bf16x8 xf[2], wf[3];
#pragma unroll
      for (int i = 0; i < 2; ++i) xf[i] = *(LAS const bf16x8*)(base + xoff[s] + i * 4096);
#pragma unroll
      for (int g = 0; g < 3; ++g) wf[g] = cat(vtr(base + woff + g * 8192 + (16 * s) * 128), vtr(base + woff + g * 8192 + (16 * s + 4) * 128));
#pragma unroll
      for (int i = 0; i < 2; ++i) { aq[i] = MFMA32(wf[0], xf[i], aq[i]); ak[i] = MFMA32(wf[1], xf[i], ak[i]); av[i] = MFMA32(xf[i], wf[2], av[i]); }
    }
  }
  const int h = 2 * hp + wn;
  float kmx = 0.f;
#pragma unroll
  for (int i = 0; i < 2; ++i) {
    const int tb = rb * 8 + wm * 2 + i;
    char* qd = ws + WS_Q + ((size_t)(b * 32 + h) * 64 + tb) * 2048 + lane * 16;
    char* kd = ws + WS_KV + ((size_t)(b * 32 + h) * 64 + tb) * 4096 + lane * 16;
    f32x16 q = aq[i] * QSCALE;
    *(u32x4*)qd = pack8(q, 0); *(u32x4*)(qd + 1024) = pack8(q, 1);
    u32x4 k0 = pack8(ak[i], 0), k1 = pack8(ak[i], 1);
    *(u32x4*)kd = k0; *(u32x4*)(kd + 1024) = k1;
    *(u32x4*)(kd + 2048) = pack8(av[i], 0); *(u32x4*)(kd + 3072) = pack8(av[i], 1);
    float n2 = sumsq8(k0) + sumsq8(k1); n2 += __shfl_xor(n2, 32); kmx = fmaxf(kmx, n2);
  }
#pragma unroll
  for (int o = 1; o < 32; o <<= 1) kmx = fmaxf(kmx, __shfl_xor(kmx, o));
  if (lane == 0) ((float*)(ws + WS_KMAX))[(b * 32 + h) * 32 + rb * 4 + wm] = kmx;
}

constexpr int A_STAGE = 16384;
__global__ __launch_bounds__(256, 2) void attn_kernel(char* __restrict__ ws) {
  __shared__ __attribute__((aligned(16))) char smem[2 * A_STAGE];
  LAS char* lds = (LAS char*)smem;
  const int tid = threadIdx.x, lane = tid & 63, wid = __builtin_amdgcn_readfirstlane(tid >> 6);
  const int xcd = blockIdx.x & 7, j = blockIdx.x >> 3;
  const int bh = xcd * 8 + (j >> 3), qb = j & 7;
  const int b = bh >> 5, h = bh & 31, hi = lane >> 5;
  const char* kv = ws + WS_KV + (size_t)bh * 64 * 4096;
  const int tb0 = qb * 8 + wid * 2;
  bf16x8 qf[2][2];
#pragma unroll
  for (int i = 0; i < 2; ++i)
#pragma unroll
    for (int s = 0; s < 2; ++s) qf[i][s] = *(const bf16x8*)(ws + WS_Q + ((size_t)bh * 64 + tb0 + i) * 2048 + s * 1024 + lane * 16);
  float km = ((const float*)(ws + WS_KMAX))[bh * 32 + (lane & 31)];
#pragma unroll
  for (int o = 1; o < 32; o <<= 1) km = fmaxf(km, __shfl_xor(km, o));
  f32x16 negm[2];
#pragma unroll
  for (int i = 0; i < 2; ++i) { float n2 = sumsq8(__builtin_bit_cast(u32x4, qf[i][0])) + sumsq8(__builtin_bit_cast(u32x4, qf[i][1])); n2 += __shfl_xor(n2, 32);
    float m = sqrtf(n2 * km) * 1.001f;
#pragma unroll
    for (int r = 0; r < 16; ++r) negm[i][r] = -m; }
  bf16x8 ones; { short v = (((lane >> 4) & 1) == (((lane & 15) >> 2) & 1)) ? (short)0x3f80 : (short)0; ones = (bf16x8){v, v, v, v, v, v, v, v}; }
  f32x16 o[2] = {}; f32x4 l[2] = {};
  auto stage = [&](int buf, int st) {
#pragma unroll
    for (int i = 0; i < 4; ++i) glds16(kv + (size_t)st * A_STAGE + (4 * wid + i) * 1024 + lane * 16, lds + buf * A_STAGE + (4 * wid + i) * 1024);
  };
  stage(0, 0);
  for (int st = 0; st < 16; ++st) {
    asm volatile("s_waitcnt vmcnt(0)" ::: "memory");
    __syncthreads();
    if (st + 1 < 16) stage((st + 1) & 1, st + 1);
    LAS char* base = lds + (st & 1) * A_STAGE + lane * 16;
#pragma unroll
    for (int kb = 0; kb < 4; ++kb) {
      bf16x8 k0 = *(LAS const bf16x8*)(base + kb * 4096), k1 = *(LAS const bf16x8*)(base + kb * 4096 + 1024);
      bf16x8 v0 = *(LAS const bf16x8*)(base + kb * 4096 + 2048), v1 = *(LAS const bf16x8*)(base + kb * 4096 + 3072);
#pragma unroll
      for (int i = 0; i < 2; ++i) {
        f32x16 s = MFMA32(k0, qf[i][0], negm[i]); s = MFMA32(k1, qf[i][1], s);
#pragma unroll
        for (int r = 0; r < 16; ++r) s[r] = __builtin_amdgcn_exp2f(s[r]);
        bf16x8 p0 = __builtin_bit_cast(bf16x8, pack8(s, 0)), p1 = __builtin_bit_cast(bf16x8, pack8(s, 1));
        o[i] = MFMA32(v0, p0, o[i]); o[i] = MFMA32(v1, p1, o[i]);
        l[i] = MFMA16(ones, p0, l[i]); l[i] = MFMA16(ones, p1, l[i]);
      }
    }
  }
#pragma unroll
  for (int i = 0; i < 2; ++i) {
    float lsum = l[i][0];
    if (__builtin_expect(__any(!(lsum >= 9.094947e-13f)), 0)) {
      float m = -INFINITY; lsum = 0.f; f32x16 oo = {};
      for (int kb = 0; kb < 64; ++kb) {
        const char* src = kv + (size_t)kb * 4096 + lane * 16;
        bf16x8 k0 = *(const bf16x8*)src, k1 = *(const bf16x8*)(src + 1024), v0 = *(const bf16x8*)(src + 2048), v1 = *(const bf16x8*)(src + 3072);
        f32x16 z = {}; f32x16 s = MFMA32(k0, qf[i][0], z); s = MFMA32(k1, qf[i][1], s);
        float mx = s[0];
#pragma unroll
        for (int r = 1; r < 16; ++r) mx = fmaxf(mx, s[r]);
        mx = fmaxf(mx, __shfl_xor(mx, 32));
        float mn = fmaxf(m, mx); float al = __builtin_amdgcn_exp2f(m - mn); m = mn;
        float rs = 0.f;
#pragma unroll
        for (int r = 0; r < 16; ++r) s[r] = __builtin_amdgcn_exp2f(s[r] - mn);
        u32x4 w0 = pack8(s, 0), w1 = pack8(s, 1);
#pragma unroll
        for (int r = 0; r < 4; ++r) { rs += __uint_as_float(w0[r] << 16) + __uint_as_float(w0[r] & 0xffff0000u); rs += __uint_as_float(w1[r] << 16) + __uint_as_float(w1[r] & 0xffff0000u); }
        rs += __shfl_xor(rs, 32);
        lsum = lsum * al + rs;
        oo = oo * al;
        oo = MFMA32(v0, __builtin_bit_cast(bf16x8, w0), oo); oo = MFMA32(v1, __builtin_bit_cast(bf16x8, w1), oo);
      }
      o[i] = oo;
    }
    const float inv = 1.0f / lsum;
    f32x16 on = o[i] * inv;
    char* od = ws + WS_O + (((size_t)b * 64 + tb0 + i) * 64 + 2 * h) * 1024 + lane * 16;
    *(u32x4*)od = pack8(on, 0); *(u32x4*)(od + 1024) = pack8(on, 1);
  }
  (void)hi;
}

constexpr int G2_STAGE = 16384 + 16384;
__global__ __launch_bounds__(512) void out_kernel(const uint16_t* __restrict__ woa, const uint16_t* __restrict__ wob, const char* __restrict__ ws, uint16_t* __restrict__ out) {
  __shared__ __attribute__((aligned(16))) char smem[2 * G2_STAGE];
  LAS char* lds = (LAS char*)smem;
  const int tid = threadIdx.x, lane = tid & 63, wid = __builtin_amdgcn_readfirstlane(tid >> 6);
  const int xcd = blockIdx.x & 7, j = blockIdx.x >> 3;
  const int b = xcd >> 2, xq = xcd & 3;
  const int rb = xq * 4 + (j & 3), cb = j >> 2;
  const uint16_t* w = b ? wob : woa;
  const int wm = wid >> 2, wn = wid & 3, hi = lane >> 5, l31 = lane & 31;
  const char* osrc[2]; const char* wsrc[2];
#pragma unroll
  for (int i = 0; i < 2; ++i) { int p = 2 * wid + i, tb = p >> 2, kk = p & 3; osrc[i] = ws + WS_O + (((size_t)b * 64 + rb * 4 + tb) * 64 + kk) * 1024 + lane * 16; }
#pragma unroll
  for (int i = 0; i < 2; ++i) { int p = 2 * wid + i, seg = p >> 3, g = p & 7, k = 8 * g + (lane >> 3), ch = (lane & 7) ^ (((k >> 1) & 1) << 2) ^ (((k >> 2) & 1) << 2); wsrc[i] = (const char*)w + (size_t)k * 2048 + (cb * 128 + seg * 64) * 2 + ch * 16; }
  auto stage = [&](int buf, int ks) {
    LAS char* base = lds + buf * G2_STAGE;
#pragma unroll
    for (int i = 0; i < 2; ++i) glds16(osrc[i] + ks * 4096, base + (2 * wid + i) * 1024);
#pragma unroll
    for (int i = 0; i < 2; ++i) glds16(wsrc[i] + (size_t)ks * 64 * 2048, base + 16384 + (2 * wid + i) * 1024);
  };
  int woff; { int g = lane >> 4, i = lane & 15, q = i >> 2, p = i & 3; int k = 4 * (g >> 1) + q;
    int colb = 16 * (g & 1) + 8 * (p & 1) + 4 * (p >> 1);
    int col = 32 * wn + colb;
    int seg = col >> 6, cs = col & 63, ch = (cs >> 3) ^ (((k >> 1) & 1) << 2) ^ (((k >> 2) & 1) << 2);
    woff = 16384 + seg * 8192 + k * 128 + ch * 16 + ((cs >> 2) & 1) * 8; }
  f32x16 acc[2] = {};
  stage(0, 0);
  for (int ks = 0; ks < 16; ++ks) {
    asm volatile("s_waitcnt vmcnt(0)" ::: "memory");
    __syncthreads();
    if (ks + 1 < 16) stage((ks + 1) & 1, ks + 1);
    LAS char* base = lds + (ks & 1) * G2_STAGE;
#pragma unroll
    for (int s = 0; s < 4; ++s) {
      bf16x8 wf = cat(vtr(base + woff + (16 * s) * 128), vtr(base + woff + (16 * s + 8) * 128));
#pragma unroll
      for (int i = 0; i < 2; ++i) { bf16x8 of = *(LAS const bf16x8*)(base + ((2 * wm + i) * 4 + s) * 1024 + lane * 16); acc[i] = MFMA32(wf, of, acc[i]); }
    }
  }
#pragma unroll
  for (int i = 0; i < 2; ++i) {
    const int t = rb * 128 + wm * 64 + i * 32 + l31;
    uint16_t* dst = out + (size_t)t * 2048 + b * 1024 + cb * 128 + wn * 32 + 8 * hi;
    *(u32x4*)dst = pack8(acc[i], 0); *(u32x4*)(dst + 16) = pack8(acc[i], 1);
  }
}

extern "C" void kernel_launch(void* const* d_in, const int* in_sizes, int n_in, void* d_out, int out_size, void* d_ws, size_t ws_size, hipStream_t stream) {
  const uint16_t* xa = (const uint16_t*)d_in[0]; const uint16_t* wqa = (const uint16_t*)d_in[1]; const uint16_t* woa = (const uint16_t*)d_in[2];
  const uint16_t* xb = (const uint16_t*)d_in[3]; const uint16_t* wqb = (const uint16_t*)d_in[4]; const uint16_t* wob = (const uint16_t*)d_in[5];
  char* ws = (char*)d_ws;
  qkv_kernel<<<256, 512, 0, stream>>>(xa, wqa, xb, wqb, ws);
  attn_kernel<<<512, 256, 0, stream>>>(ws);
  out_kernel<<<256, 512, 0, stream>>>(woa, wob, ws, (uint16_t*)d_out);
}
```

```cpp
#include <hip/hip_runtime.h>
#include <hip/hip_bf16.h>
#include <stdint.h>

typedef float f32x16 __attribute__((ext_vector_type(16)));
typedef float f32x4 __attribute__((ext_vector_type(4)));
typedef float f32x2 __attribute__((ext_vector_type(2)));
typedef short bf16x8 __attribute__((ext_vector_type(8)));
typedef __bf16 bf16x2_t __attribute__((ext_vector_type(2)));
typedef unsigned u32x4 __attribute__((ext_vector_type(4)));
#define LAS __attribute__((address_space(3)))
#define GAS __attribute__((address_space(1)))
#define DI __device__ __forceinline__
#define MFMA32(a, b, c) __builtin_amdgcn_mfma_f32_32x32x16_bf16(a, b, c, 0, 0, 0)

constexpr size_t WS_KV = 0;
constexpr size_t WS_O = 16u << 20;
constexpr size_t WS_CTL = 24u << 20;
constexpr size_t WS_FKV = WS_CTL + 4096;
constexpr size_t WS_FO = WS_FKV + 8192;
constexpr float QSCALE = 0.17677669529663687f * 1.4426950408889634f;
constexpr int G1_STAGE = 65536;
constexpr int AUX_SC1 = 16;
constexpr int OUT_AUX = 0;

DI unsigned cvtpk(float lo, float hi) { f32x2 v = {lo, hi}; bf16x2_t b = __builtin_convertvector(v, bf16x2_t); return __builtin_bit_cast(unsigned, b); }
DI int lane_id() { int l; asm volatile("v_mbcnt_lo_u32_b32 %0, -1, 0\n\tv_mbcnt_hi_u32_b32 %0, -1, %0" : "=v"(l)); return l; }
DI u32x4 pack8(const f32x16& a, int s) { u32x4 w; w[0] = cvtpk(a[8*s+0], a[8*s+1]); w[1] = cvtpk(a[8*s+2], a[8*s+3]); w[2] = cvtpk(a[8*s+4], a[8*s+5]); w[3] = cvtpk(a[8*s+6], a[8*s+7]); return w; }
DI u32x4 pack44(const f32x16& a, const f32x16& b, int t, float sc) {
  u32x4 w; w[0] = cvtpk(a[4*t+0] * sc, a[4*t+1] * sc); w[1] = cvtpk(a[4*t+2] * sc, a[4*t+3] * sc); w[2] = cvtpk(b[4*t+0] * sc, b[4*t+1] * sc); w[3] = cvtpk(b[4*t+2] * sc, b[4*t+3] * sc); return w; }
DI u32x4 pack44t(const f32x16& a, int t0, int t1) {
  u32x4 w; w[0] = cvtpk(a[4*t0+0], a[4*t0+1]); w[1] = cvtpk(a[4*t0+2], a[4*t0+3]); w[2] = cvtpk(a[4*t1+0], a[4*t1+1]); w[3] = cvtpk(a[4*t1+2], a[4*t1+3]); return w; }

typedef unsigned u32x8 __attribute__((ext_vector_type(8)));
DI const char* mkptr(unsigned lo, unsigned hi) { return (const char*)(((unsigned long long)hi << 32) | lo); }
__global__ __launch_bounds__(512, 2) void mha_fused(const uint16_t* __restrict__ xa_, const uint16_t* __restrict__ wa_, const uint16_t* __restrict__ woa_,
                                                 const uint16_t* __restrict__ xb_, const uint16_t* __restrict__ wb_, const uint16_t* __restrict__ wob_,
                                                 char* __restrict__ ws_, uint16_t* __restrict__ out_) {
  u32x8 ka0, ka1;
  asm volatile("s_load_dwordx8 %0, %2, 0x0\n\ts_load_dwordx8 %1, %2, 0x20" : "=&s"(ka0), "=&s"(ka1) : "s"(__builtin_amdgcn_kernarg_segment_ptr()));
  __shared__ __attribute__((aligned(16))) char smem[131072];
  LAS char* lds = (LAS char*)smem;
  const int tid = threadIdx.x, lane = tid & 63, wid = __builtin_amdgcn_readfirstlane(tid >> 6);
  const int xcd = blockIdx.x & 7, j = blockIdx.x >> 3;
  const int b = xcd >> 2, hp = 4 * (xcd & 3) + (j >> 3), rb = j & 7;
  const int wm = wid >> 1, wn = wid & 1, g4 = lane >> 4, l15 = lane & 15;
  const unsigned ldsb = (unsigned)(uintptr_t)lds;
  unsigned tag;
  const int h = 2 * hp + wn;
  bf16x8 qf[2][2];
  f32x16 o[2] = {}; f32x4 l[2] = {};
    unsigned vo[8];
    {
      const unsigned mx = wid < 4 ? 0xffffffffu : 0u;
      const unsigned stride = wid < 4 ? 2048u : 6144u, rbase = wid < 4 ? (unsigned)wid * (64u * 2048u) : (unsigned)(wid - 4) * 2048u;
      const unsigned r = lane >> 3, tk = ((r >> 1) & mx) | ((((r >> 1) & 1) << 1) & ~mx), c0 = (lane & 7) ^ tk, o0 = r * stride + rbase;
#pragma unroll
      for (int i = 0; i < 8; ++i) vo[i] = o0 + (unsigned)(8 * i) * stride - (unsigned)((i & 3) * 1024) + ((c0 ^ (unsigned)(4 * (i & 1))) * 16u);
      const unsigned m7 = wid == 7 ? 0xffffffffu : 0u, jb = hp * 8 + rb;
      const unsigned t1 = lane < 24 ? (24u * jb + lane) * 128u : 0x80000000u;
      vo[1] = (t1 & m7) | (vo[1] & ~m7);
#pragma unroll
      for (int i = 0; i < 4; ++i) { const unsigned tx_ = (16u * jb + 8u * (i & 1) + (lane >> 3)) * 2048u + (8u * (i >> 1) + (lane & 7)) * 128u; vo[2 + i] = (tx_ & m7) | (vo[2 + i] & ~m7); }
    }
    unsigned xoff[2];
#pragma unroll
    for (int s = 0; s < 2; ++s) { int row = 64 * wm + l15; xoff[s] = ldsb + row * 128 + (((4 * s + g4) ^ ((row >> 1) & 7)) * 16); }
    unsigned woff[2];
#pragma unroll
    for (int hf = 0; hf < 2; ++hf) { int q = l15 >> 2, p = l15 & 3, k = 8 * g4 + q, f = ((k >> 1) & 1) | (((k >> 3) & 1) << 1), ch = (4 * wn + 2 * hf + (p >> 1)) ^ (f << 1);
      woff[hf] = ldsb + 32768 + k * 128 + ch * 16 + (p & 1) * 8; }
    const unsigned lbase = __builtin_amdgcn_readfirstlane(ldsb + wid * 8192);
  asm volatile("" :: "v"(vo[0]), "v"(vo[1]), "v"(vo[2]), "v"(vo[3]), "v"(vo[4]), "v"(vo[5]), "v"(vo[6]), "v"(vo[7]), "v"(xoff[0]), "v"(xoff[1]), "v"(woff[0]), "v"(woff[1]));
  asm volatile("s_waitcnt lgkmcnt(0)" : "+s"(ka0), "+s"(ka1));
  const uint16_t* xa = (const uint16_t*)mkptr(ka0[0], ka0[1]); const uint16_t* wa = (const uint16_t*)mkptr(ka0[2], ka0[3]); const uint16_t* woa = (const uint16_t*)mkptr(ka0[4], ka0[5]);
  const uint16_t* xb = (const uint16_t*)mkptr(ka0[6], ka0[7]); const uint16_t* wb = (const uint16_t*)mkptr(ka1[0], ka1[1]); const uint16_t* wob = (const uint16_t*)mkptr(ka1[2], ka1[3]);
  char* ws = (char*)mkptr(ka1[4], ka1[5]); uint16_t* out = (uint16_t*)mkptr(ka1[6], ka1[7]);
  GAS unsigned* ctl = (GAS unsigned*)(ws + WS_CTL) + b * 64;
  {
    const uint16_t* x = b ? xb : xa; const uint16_t* w = b ? wb : wa;
    const __amdgpu_buffer_rsrc_t xr = __builtin_amdgcn_make_buffer_rsrc((void*)(x + (size_t)rb * 256 * 1024), (short)0, 256 * 2048, 0x00020000);
    const __amdgpu_buffer_rsrc_t wr = __builtin_amdgcn_make_buffer_rsrc((void*)(w + 64 * hp), (short)0, 1024 * 6144 - 128 * hp, 0x00020000);
    const __amdgpu_buffer_rsrc_t xrt = __builtin_amdgcn_make_buffer_rsrc((void*)x, (short)0, 2048 * 2048, 0x00020000);
    const __amdgpu_buffer_rsrc_t wrt = __builtin_amdgcn_make_buffer_rsrc((void*)w, (short)0, 1024 * 6144, 0x00020000);
    const __amdgpu_buffer_rsrc_t ldr = wid < 4 ? xr : wr;
    const unsigned sst = wid < 4 ? 128u : 64u * 6144u;

    f32x16 A0, A1, A2, A3, A4, A5;
    asm volatile("global_load_dword %0, %1, off sc1" : "=v"(tag) : "v"(ctl) : "memory");
    asm volatile(
    "s_nop 4\n"
    "s_sub_u32 s40, 0, %22\n"
    "s_cmp_eq_u32 %23, 7\n"
    "s_cbranch_scc1 Lpt_%=\n"
    "s_add_u32 s40, s40, %22\n"
    "s_add_u32 m0, %21, 0\n"
    "s_nop 0\n"
    "buffer_load_dwordx4 %10, %18, s40 offen offset:0 lds\n"
    "buffer_load_dwordx4 %11, %18, s40 offen offset:1024 lds\n"
    "buffer_load_dwordx4 %12, %18, s40 offen offset:2048 lds\n"
    "buffer_load_dwordx4 %13, %18, s40 offen offset:3072 lds\n"
    "s_add_u32 m0, %21, 4096\n"
    "s_nop 0\n"
    "buffer_load_dwordx4 %14, %18, s40 offen offset:0 lds\n"
    "buffer_load_dwordx4 %15, %18, s40 offen offset:1024 lds\n"
    "buffer_load_dwordx4 %16, %18, s40 offen offset:2048 lds\n"
    "buffer_load_dwordx4 %17, %18, s40 offen offset:3072 lds\n"
    "v_mov_b32 v16, 0\n"
    "v_mov_b32 v17, 0\n"
    "v_mov_b32 v18, 0\n"
    "v_mov_b32 v19, 0\n"
    "v_mov_b32 v20, 0\n"
    "v_mov_b32 v21, 0\n"
    "v_mov_b32 v22, 0\n"
    "v_mov_b32 v23, 0\n"
    "v_mov_b32 v24, 0\n"
    "v_mov_b32 v25, 0\n"
    "v_mov_b32 v26, 0\n"
    "v_mov_b32 v27, 0\n"
    "v_mov_b32 v28, 0\n"
    "v_mov_b32 v29, 0\n"
    "v_mov_b32 v30, 0\n"
    "v_mov_b32 v31, 0\n"
    "v_mov_b32 v32, 0\n"
    "v_mov_b32 v33, 0\n"
    "v_mov_b32 v34, 0\n"
    "v_mov_b32 v35, 0\n"
    "v_mov_b32 v36, 0\n"
    "v_mov_b32 v37, 0\n"
    "v_mov_b32 v38, 0\n"
    "v_mov_b32 v39, 0\n"
    "v_mov_b32 v40, 0\n"
    "v_mov_b32 v41, 0\n"
    "v_mov_b32 v42, 0\n"
    "v_mov_b32 v43, 0\n"
    "v_mov_b32 v44, 0\n"
    "v_mov_b32 v45, 0\n"
    "v_mov_b32 v46, 0\n"
    "v_mov_b32 v47, 0\n"
    "v_mov_b32 v48, 0\n"
    "v_mov_b32 v49, 0\n"
    "v_mov_b32 v50, 0\n"
    "v_mov_b32 v51, 0\n"
    "v_mov_b32 v52, 0\n"
    "v_mov_b32 v53, 0\n"
    "v_mov_b32 v54, 0\n"
    "v_mov_b32 v55, 0\n"
    "v_mov_b32 v56, 0\n"
    "v_mov_b32 v57, 0\n"
    "v_mov_b32 v58, 0\n"
    "v_mov_b32 v59, 0\n"
    "v_mov_b32 v60, 0\n"
    "v_mov_b32 v61, 0\n"
    "v_mov_b32 v62, 0\n"
    "v_mov_b32 v63, 0\n"
    "v_mov_b32 v64, 0\n"
    "v_mov_b32 v65, 0\n"
    "v_mov_b32 v66, 0\n"
    "v_mov_b32 v67, 0\n"
    "v_mov_b32 v68, 0\n"
    "v_mov_b32 v69, 0\n"
    "v_mov_b32 v70, 0\n"
    "v_mov_b32 v71, 0\n"
    "v_mov_b32 v72, 0\n"
    "v_mov_b32 v73, 0\n"
    "v_mov_b32 v74, 0\n"
    "v_mov_b32 v75, 0\n"
    "v_mov_b32 v76, 0\n"
    "v_mov_b32 v77, 0\n"
    "v_mov_b32 v78, 0\n"
    "v_mov_b32 v79, 0\n"
    "v_mov_b32 v80, 0\n"
    "v_mov_b32 v81, 0\n"
    "v_mov_b32 v82, 0\n"
    "v_mov_b32 v83, 0\n"
    "v_mov_b32 v84, 0\n"
    "v_mov_b32 v85, 0\n"
    "v_mov_b32 v86, 0\n"
    "v_mov_b32 v87, 0\n"
    "v_mov_b32 v88, 0\n"
    "v_mov_b32 v89, 0\n"
    "v_mov_b32 v90, 0\n"
    "v_mov_b32 v91, 0\n"
    "v_mov_b32 v92, 0\n"
    "v_mov_b32 v93, 0\n"
    "v_mov_b32 v94, 0\n"
    "v_mov_b32 v95, 0\n"
    "v_mov_b32 v96, 0\n"
    "v_mov_b32 v97, 0\n"
    "v_mov_b32 v98, 0\n"
    "v_mov_b32 v99, 0\n"
    "v_mov_b32 v100, 0\n"
    "v_mov_b32 v101, 0\n"
    "v_mov_b32 v102, 0\n"
    "v_mov_b32 v103, 0\n"
    "v_mov_b32 v104, 0\n"
    "v_mov_b32 v105, 0\n"
    "v_mov_b32 v106, 0\n"
    "v_mov_b32 v107, 0\n"
    "v_mov_b32 v108, 0\n"
    "v_mov_b32 v109, 0\n"
    "v_mov_b32 v110, 0\n"
    "v_mov_b32 v111, 0\n"
    "s_add_u32 s40, s40, %22\n"
    "s_add_u32 m0, %21, 65536\n"
    "s_nop 0\n"
    "buffer_load_dwordx4 %10, %18, s40 offen offset:0 lds\n"
    "buffer_load_dwordx4 %11, %18, s40 offen offset:1024 lds\n"
    "buffer_load_dwordx4 %12, %18, s40 offen offset:2048 lds\n"
    "buffer_load_dwordx4 %13, %18, s40 offen offset:3072 lds\n"
    "s_add_u32 m0, %21, 69632\n"
    "s_nop 0\n"
    "buffer_load_dwordx4 %14, %18, s40 offen offset:0 lds\n"
    "buffer_load_dwordx4 %15, %18, s40 offen offset:1024 lds\n"
    "buffer_load_dwordx4 %16, %18, s40 offen offset:2048 lds\n"
    "buffer_load_dwordx4 %17, %18, s40 offen offset:3072 lds\n"
    "s_branch Lpj_%=\n"
    "Lpt_%=:\n"
    "s_mov_b32 s43, 128\n"
    "s_mov_b32 s52, 393216\n"
    "v_mov_b32 v16, 0\n"
    "v_mov_b32 v17, 0\n"
    "v_mov_b32 v18, 0\n"
    "v_mov_b32 v19, 0\n"
    "v_mov_b32 v20, 0\n"
    "v_mov_b32 v21, 0\n"
    "v_mov_b32 v22, 0\n"
    "v_mov_b32 v23, 0\n"
    "v_mov_b32 v24, 0\n"
    "v_mov_b32 v25, 0\n"
    "v_mov_b32 v26, 0\n"
    "v_mov_b32 v27, 0\n"
    "v_mov_b32 v28, 0\n"
    "v_mov_b32 v29, 0\n"
    "v_mov_b32 v30, 0\n"
    "v_mov_b32 v31, 0\n"
    "v_mov_b32 v32, 0\n"
    "v_mov_b32 v33, 0\n"
    "v_mov_b32 v34, 0\n"
    "v_mov_b32 v35, 0\n"
    "v_mov_b32 v36, 0\n"
    "v_mov_b32 v37, 0\n"
    "v_mov_b32 v38, 0\n"
    "v_mov_b32 v39, 0\n"
    "v_mov_b32 v40, 0\n"
    "v_mov_b32 v41, 0\n"
    "v_mov_b32 v42, 0\n"
    "v_mov_b32 v43, 0\n"
    "v_mov_b32 v44, 0\n"
    "v_mov_b32 v45, 0\n"
    "v_mov_b32 v46, 0\n"
    "v_mov_b32 v47, 0\n"
    "v_mov_b32 v48, 0\n"
    "v_mov_b32 v49, 0\n"
    "v_mov_b32 v50, 0\n"
    "v_mov_b32 v51, 0\n"
    "v_mov_b32 v52, 0\n"
    "v_mov_b32 v53, 0\n"
    "v_mov_b32 v54, 0\n"
    "v_mov_b32 v55, 0\n"
    "v_mov_b32 v56, 0\n"
    "v_mov_b32 v57, 0\n"
    "v_mov_b32 v58, 0\n"
    "v_mov_b32 v59, 0\n"
    "v_mov_b32 v60, 0\n"
    "v_mov_b32 v61, 0\n"
    "v_mov_b32 v62, 0\n"
    "v_mov_b32 v63, 0\n"
    "v_mov_b32 v64, 0\n"
    "v_mov_b32 v65, 0\n"
    "v_mov_b32 v66, 0\n"
    "v_mov_b32 v67, 0\n"
    "v_mov_b32 v68, 0\n"
    "v_mov_b32 v69, 0\n"
    "v_mov_b32 v70, 0\n"
    "v_mov_b32 v71, 0\n"
    "v_mov_b32 v72, 0\n"
    "v_mov_b32 v73, 0\n"
    "v_mov_b32 v74, 0\n"
    "v_mov_b32 v75, 0\n"
    "v_mov_b32 v76, 0\n"
    "v_mov_b32 v77, 0\n"
    "v_mov_b32 v78, 0\n"
    "v_mov_b32 v79, 0\n"
    "v_mov_b32 v80, 0\n"
    "v_mov_b32 v81, 0\n"
    "v_mov_b32 v82, 0\n"
    "v_mov_b32 v83, 0\n"
    "v_mov_b32 v84, 0\n"
    "v_mov_b32 v85, 0\n"
    "v_mov_b32 v86, 0\n"
    "v_mov_b32 v87, 0\n"
    "v_mov_b32 v88, 0\n"
    "v_mov_b32 v89, 0\n"
    "v_mov_b32 v90, 0\n"
    "v_mov_b32 v91, 0\n"
    "v_mov_b32 v92, 0\n"
    "v_mov_b32 v93, 0\n"
    "v_mov_b32 v94, 0\n"
    "v_mov_b32 v95, 0\n"
    "v_mov_b32 v96, 0\n"
    "v_mov_b32 v97, 0\n"
    "v_mov_b32 v98, 0\n"
    "v_mov_b32 v99, 0\n"
    "v_mov_b32 v100, 0\n"
    "v_mov_b32 v101, 0\n"
    "v_mov_b32 v102, 0\n"
    "v_mov_b32 v103, 0\n"
    "v_mov_b32 v104, 0\n"
    "v_mov_b32 v105, 0\n"
    "v_mov_b32 v106, 0\n"
    "v_mov_b32 v107, 0\n"
    "v_mov_b32 v108, 0\n"
    "v_mov_b32 v109, 0\n"
    "v_mov_b32 v110, 0\n"
    "v_mov_b32 v111, 0\n"
    "s_add_u32 s52, s52, 393216\n"
    "s_min_u32 s52, s52, 5898240\n"
    "buffer_load_dword v193, %11, %20, s52 offen\n"
    "s_add_u32 s52, s52, 393216\n"
    "s_min_u32 s52, s52, 5898240\n"
    "buffer_load_dword v193, %11, %20, s52 offen\n"
    "buffer_load_dword v192, %12, %19, 0 offen\n"
    "buffer_load_dword v192, %13, %19, 0 offen\n"
    "s_add_u32 s52, s52, 393216\n"
    "s_min_u32 s52, s52, 5898240\n"
    "buffer_load_dword v193, %11, %20, s52 offen\n"
    "s_add_u32 s52, s52, 393216\n"
    "s_min_u32 s52, s52, 5898240\n"
    "buffer_load_dword v193, %11, %20, s52 offen\n"
    "Lpj_%=:\n"
    "v_add_u32 v194, 65536, %6\n"
    "v_add_u32 v195, 65536, %7\n"
    "v_add_u32 v196, 65536, %8\n"
    "v_add_u32 v197, 65536, %9\n"
    "s_mov_b32 s42, 6\n"
    "s_cmp_eq_u32 %23, 7\n"
    "s_cbranch_scc1 Lpw_%=\n"
    "s_waitcnt vmcnt(8)\n"
    "Lpw_%=:\n"
    "s_barrier\n"
    "s_cmp_eq_u32 %23, 7\n"
    "s_cbranch_scc0 Lx2_%=\n"
    "buffer_load_dword v192, %14, %19, 0 offen\n"
    "buffer_load_dword v192, %15, %19, 0 offen\n"
    "Lx2_%=:\n"
    "ds_read_b128 v[112:115], %6 offset:0\n"
    "ds_read_b64_tr_b16 v[128:129], %8 offset:0\n"
    "ds_read_b64_tr_b16 v[130:131], %8 offset:512\n"
    "ds_read_b128 v[116:119], %6 offset:2048\n"
    "ds_read_b64_tr_b16 v[132:133], %9 offset:0\n"
    "ds_read_b64_tr_b16 v[134:135], %9 offset:512\n"
    "ds_read_b128 v[120:123], %6 offset:4096\n"
    "ds_read_b64_tr_b16 v[136:137], %8 offset:8192\n"
    "ds_read_b64_tr_b16 v[138:139], %8 offset:8704\n"
    "ds_read_b128 v[124:127], %6 offset:6144\n"
    "ds_read_b64_tr_b16 v[140:141], %9 offset:8192\n"
    "ds_read_b64_tr_b16 v[142:143], %9 offset:8704\n"
    "ds_read_b64_tr_b16 v[144:145], %8 offset:16384\n"
    "ds_read_b64_tr_b16 v[146:147], %8 offset:16896\n"
    "ds_read_b64_tr_b16 v[148:149], %9 offset:16384\n"
    "ds_read_b64_tr_b16 v[150:151], %9 offset:16896\n"
    "Lqkv_loop_%=:\n"
    "s_waitcnt lgkmcnt(0)\n"
    "v_mfma_f32_16x16x32_bf16 v[16:19], v[128:131], v[112:115], v[16:19]\n"
    "ds_read_b128 v[152:155], %7 offset:0\n"
    "v_mfma_f32_16x16x32_bf16 v[20:23], v[128:131], v[116:119], v[20:23]\n"
    "ds_read_b64_tr_b16 v[168:169], %8 offset:4096\n"
    "v_mfma_f32_16x16x32_bf16 v[24:27], v[128:131], v[120:123], v[24:27]\n"
    "ds_read_b64_tr_b16 v[170:171], %8 offset:4608\n"
    "v_mfma_f32_16x16x32_bf16 v[28:31], v[128:131], v[124:127], v[28:31]\n"
    "ds_read_b128 v[156:159], %7 offset:2048\n"
    "v_mfma_f32_16x16x32_bf16 v[32:35], v[132:135], v[112:115], v[32:35]\n"
    "ds_read_b64_tr_b16 v[172:173], %9 offset:4096\n"
    "v_mfma_f32_16x16x32_bf16 v[36:39], v[132:135], v[116:119], v[36:39]\n"
    "ds_read_b64_tr_b16 v[174:175], %9 offset:4608\n"
    "v_mfma_f32_16x16x32_bf16 v[40:43], v[132:135], v[120:123], v[40:43]\n"
    "ds_read_b128 v[160:163], %7 offset:4096\n"
    "v_mfma_f32_16x16x32_bf16 v[44:47], v[132:135], v[124:127], v[44:47]\n"
    "ds_read_b64_tr_b16 v[176:177], %8 offset:12288\n"
    "v_mfma_f32_16x16x32_bf16 v[48:51], v[136:139], v[112:115], v[48:51]\n"
    "ds_read_b64_tr_b16 v[178:179], %8 offset:12800\n"
    "v_mfma_f32_16x16x32_bf16 v[52:55], v[136:139], v[116:119], v[52:55]\n"
    "ds_read_b128 v[164:167], %7 offset:6144\n"
    "v_mfma_f32_16x16x32_bf16 v[56:59], v[136:139], v[120:123], v[56:59]\n"
    "ds_read_b64_tr_b16 v[180:181], %9 offset:12288\n"
    "v_mfma_f32_16x16x32_bf16 v[60:63], v[136:139], v[124:127], v[60:63]\n"
    "ds_read_b64_tr_b16 v[182:183], %9 offset:12800\n"
    "v_mfma_f32_16x16x32_bf16 v[64:67], v[140:143], v[112:115], v[64:67]\n"
    "ds_read_b64_tr_b16 v[184:185], %8 offset:20480\n"
    "v_mfma_f32_16x16x32_bf16 v[68:71], v[140:143], v[116:119], v[68:71]\n"
    "ds_read_b64_tr_b16 v[186:187], %8 offset:20992\n"
    "v_mfma_f32_16x16x32_bf16 v[72:75], v[140:143], v[120:123], v[72:75]\n"
    "ds_read_b64_tr_b16 v[188:189], %9 offset:20480\n"
    "v_mfma_f32_16x16x32_bf16 v[76:79], v[140:143], v[124:127], v[76:79]\n"
    "ds_read_b64_tr_b16 v[190:191], %9 offset:20992\n"
    "v_mfma_f32_16x16x32_bf16 v[80:83], v[112:115], v[144:147], v[80:83]\n"
    "v_mfma_f32_16x16x32_bf16 v[84:87], v[116:119], v[144:147], v[84:87]\n"
    "v_mfma_f32_16x16x32_bf16 v[88:91], v[120:123], v[144:147], v[88:91]\n"
    "v_mfma_f32_16x16x32_bf16 v[92:95], v[124:127], v[144:147], v[92:95]\n"
    "v_mfma_f32_16x16x32_bf16 v[96:99], v[112:115], v[148:151], v[96:99]\n"
    "v_mfma_f32_16x16x32_bf16 v[100:103], v[116:119], v[148:151], v[100:103]\n"
    "v_mfma_f32_16x16x32_bf16 v[104:107], v[120:123], v[148:151], v[104:107]\n"
    "v_mfma_f32_16x16x32_bf16 v[108:111], v[124:127], v[148:151], v[108:111]\n"
    "s_cmp_eq_u32 %23, 7\n"
    "s_cbranch_scc1 Lt0_%=\n"
    "s_waitcnt vmcnt(0) lgkmcnt(0)\n"
    "s_barrier\n"
    "v_mfma_f32_16x16x32_bf16 v[16:19], v[168:171], v[152:155], v[16:19]\n"
    "ds_read_b128 v[112:115], v194 offset:0\n"
    "v_mfma_f32_16x16x32_bf16 v[20:23], v[168:171], v[156:159], v[20:23]\n"
    "ds_read_b64_tr_b16 v[128:129], v196 offset:0\n"
    "v_mfma_f32_16x16x32_bf16 v[24:27], v[168:171], v[160:163], v[24:27]\n"
    "ds_read_b64_tr_b16 v[130:131], v196 offset:512\n"
    "v_mfma_f32_16x16x32_bf16 v[28:31], v[168:171], v[164:167], v[28:31]\n"
    "ds_read_b128 v[116:119], v194 offset:2048\n"
    "v_mfma_f32_16x16x32_bf16 v[32:35], v[172:175], v[152:155], v[32:35]\n"
    "ds_read_b64_tr_b16 v[132:133], v197 offset:0\n"
    "s_add_u32 s40, s40, %22\n"
    "v_mfma_f32_16x16x32_bf16 v[36:39], v[172:175], v[156:159], v[36:39]\n"
    "ds_read_b64_tr_b16 v[134:135], v197 offset:512\n"
    "s_add_u32 m0, %21, 0\n"
    "v_mfma_f32_16x16x32_bf16 v[40:43], v[172:175], v[160:163], v[40:43]\n"
    "ds_read_b128 v[120:123], v194 offset:4096\n"
    "s_nop 0\n"
    "v_mfma_f32_16x16x32_bf16 v[44:47], v[172:175], v[164:167], v[44:47]\n"
    "ds_read_b64_tr_b16 v[136:137], v196 offset:8192\n"
    "buffer_load_dwordx4 %10, %18, s40 offen offset:0 lds\n"
    "v_mfma_f32_16x16x32_bf16 v[48:51], v[176:179], v[152:155], v[48:51]\n"
    "ds_read_b64_tr_b16 v[138:139], v196 offset:8704\n"
    "buffer_load_dwordx4 %11, %18, s40 offen offset:1024 lds\n"
    "v_mfma_f32_16x16x32_bf16 v[52:55], v[176:179], v[156:159], v[52:55]\n"
    "ds_read_b128 v[124:127], v194 offset:6144\n"
    "buffer_load_dwordx4 %12, %18, s40 offen offset:2048 lds\n"
    "v_mfma_f32_16x16x32_bf16 v[56:59], v[176:179], v[160:163], v[56:59]\n"
    "ds_read_b64_tr_b16 v[140:141], v197 offset:8192\n"
    "buffer_load_dwordx4 %13, %18, s40 offen offset:3072 lds\n"
    "v_mfma_f32_16x16x32_bf16 v[60:63], v[176:179], v[164:167], v[60:63]\n"
    "ds_read_b64_tr_b16 v[142:143], v197 offset:8704\n"
    "s_add_u32 m0, %21, 4096\n"
    "v_mfma_f32_16x16x32_bf16 v[64:67], v[180:183], v[152:155], v[64:67]\n"
    "ds_read_b64_tr_b16 v[144:145], v196 offset:16384\n"
    "s_nop 0\n"
    "v_mfma_f32_16x16x32_bf16 v[68:71], v[180:183], v[156:159], v[68:71]\n"
    "ds_read_b64_tr_b16 v[146:147], v196 offset:16896\n"
    "buffer_load_dwordx4 %14, %18, s40 offen offset:0 lds\n"
    "v_mfma_f32_16x16x32_bf16 v[72:75], v[180:183], v[160:163], v[72:75]\n"
    "ds_read_b64_tr_b16 v[148:149], v197 offset:16384\n"
    "buffer_load_dwordx4 %15, %18, s40 offen offset:1024 lds\n"
    "v_mfma_f32_16x16x32_bf16 v[76:79], v[180:183], v[164:167], v[76:79]\n"
    "ds_read_b64_tr_b16 v[150:151], v197 offset:16896\n"
    "buffer_load_dwordx4 %16, %18, s40 offen offset:2048 lds\n"
    "v_mfma_f32_16x16x32_bf16 v[80:83], v[152:155], v[184:187], v[80:83]\n"
    "buffer_load_dwordx4 %17, %18, s40 offen offset:3072 lds\n"
    "v_mfma_f32_16x16x32_bf16 v[84:87], v[156:159], v[184:187], v[84:87]\n"
    "v_mfma_f32_16x16x32_bf16 v[88:91], v[160:163], v[184:187], v[88:91]\n"
    "v_mfma_f32_16x16x32_bf16 v[92:95], v[164:167], v[184:187], v[92:95]\n"
    "v_mfma_f32_16x16x32_bf16 v[96:99], v[152:155], v[188:191], v[96:99]\n"
    "v_mfma_f32_16x16x32_bf16 v[100:103], v[156:159], v[188:191], v[100:103]\n"
    "v_mfma_f32_16x16x32_bf16 v[104:107], v[160:163], v[188:191], v[104:107]\n"
    "v_mfma_f32_16x16x32_bf16 v[108:111], v[164:167], v[188:191], v[108:111]\n"
    "s_branch Lj0_%=\n"
    "Lt0_%=:\n"
    "s_waitcnt lgkmcnt(0)\n"
    "s_barrier\n"
    "v_mfma_f32_16x16x32_bf16 v[16:19], v[168:171], v[152:155], v[16:19]\n"
    "ds_read_b128 v[112:115], v194 offset:0\n"
    "v_mfma_f32_16x16x32_bf16 v[20:23], v[168:171], v[156:159], v[20:23]\n"
    "ds_read_b64_tr_b16 v[128:129], v196 offset:0\n"
    "v_mfma_f32_16x16x32_bf16 v[24:27], v[168:171], v[160:163], v[24:27]\n"
    "ds_read_b64_tr_b16 v[130:131], v196 offset:512\n"
    "v_mfma_f32_16x16x32_bf16 v[28:31], v[168:171], v[164:167], v[28:31]\n"
    "ds_read_b128 v[116:119], v194 offset:2048\n"
    "v_mfma_f32_16x16x32_bf16 v[32:35], v[172:175], v[152:155], v[32:35]\n"
    "ds_read_b64_tr_b16 v[132:133], v197 offset:0\n"
    "s_add_u32 s52, s52, 393216\n"
    "v_mfma_f32_16x16x32_bf16 v[36:39], v[172:175], v[156:159], v[36:39]\n"
    "ds_read_b64_tr_b16 v[134:135], v197 offset:512\n"
    "s_min_u32 s52, s52, 5898240\n"
    "v_mfma_f32_16x16x32_bf16 v[40:43], v[172:175], v[160:163], v[40:43]\n"
    "ds_read_b128 v[120:123], v194 offset:4096\n"
    "buffer_load_dword v193, %11, %20, s52 offen\n"
    "v_mfma_f32_16x16x32_bf16 v[44:47], v[172:175], v[164:167], v[44:47]\n"
    "ds_read_b64_tr_b16 v[136:137], v196 offset:8192\n"
    "v_mfma_f32_16x16x32_bf16 v[48:51], v[176:179], v[152:155], v[48:51]\n"
    "ds_read_b64_tr_b16 v[138:139], v196 offset:8704\n"
    "v_mfma_f32_16x16x32_bf16 v[52:55], v[176:179], v[156:159], v[52:55]\n"
    "ds_read_b128 v[124:127], v194 offset:6144\n"
    "v_mfma_f32_16x16x32_bf16 v[56:59], v[176:179], v[160:163], v[56:59]\n"
    "ds_read_b64_tr_b16 v[140:141], v197 offset:8192\n"
    "v_mfma_f32_16x16x32_bf16 v[60:63], v[176:179], v[164:167], v[60:63]\n"
    "ds_read_b64_tr_b16 v[142:143], v197 offset:8704\n"
    "v_mfma_f32_16x16x32_bf16 v[64:67], v[180:183], v[152:155], v[64:67]\n"
    "ds_read_b64_tr_b16 v[144:145], v196 offset:16384\n"
    "v_mfma_f32_16x16x32_bf16 v[68:71], v[180:183], v[156:159], v[68:71]\n"
    "ds_read_b64_tr_b16 v[146:147], v196 offset:16896\n"
    "v_mfma_f32_16x16x32_bf16 v[72:75], v[180:183], v[160:163], v[72:75]\n"
    "ds_read_b64_tr_b16 v[148:149], v197 offset:16384\n"
    "v_mfma_f32_16x16x32_bf16 v[76:79], v[180:183], v[164:167], v[76:79]\n"
    "ds_read_b64_tr_b16 v[150:151], v197 offset:16896\n"
    "v_mfma_f32_16x16x32_bf16 v[80:83], v[152:155], v[184:187], v[80:83]\n"
    "v_mfma_f32_16x16x32_bf16 v[84:87], v[156:159], v[184:187], v[84:87]\n"
    "v_mfma_f32_16x16x32_bf16 v[88:91], v[160:163], v[184:187], v[88:91]\n"
    "v_mfma_f32_16x16x32_bf16 v[92:95], v[164:167], v[184:187], v[92:95]\n"
    "v_mfma_f32_16x16x32_bf16 v[96:99], v[152:155], v[188:191], v[96:99]\n"
    "v_mfma_f32_16x16x32_bf16 v[100:103], v[156:159], v[188:191], v[100:103]\n"
    "v_mfma_f32_16x16x32_bf16 v[104:107], v[160:163], v[188:191], v[104:107]\n"
    "v_mfma_f32_16x16x32_bf16 v[108:111], v[164:167], v[188:191], v[108:111]\n"
    "Lj0_%=:\n"
    "s_waitcnt lgkmcnt(0)\n"
    "v_mfma_f32_16x16x32_bf16 v[16:19], v[128:131], v[112:115], v[16:19]\n"
    "ds_read_b128 v[152:155], v195 offset:0\n"
    "v_mfma_f32_16x16x32_bf16 v[20:23], v[128:131], v[116:119], v[20:23]\n"
    "ds_read_b64_tr_b16 v[168:169], v196 offset:4096\n"
    "v_mfma_f32_16x16x32_bf16 v[24:27], v[128:131], v[120:123], v[24:27]\n"
    "ds_read_b64_tr_b16 v[170:171], v196 offset:4608\n"
    "v_mfma_f32_16x16x32_bf16 v[28:31], v[128:131], v[124:127], v[28:31]\n"
    "ds_read_b128 v[156:159], v195 offset:2048\n"
    "v_mfma_f32_16x16x32_bf16 v[32:35], v[132:135], v[112:115], v[32:35]\n"
    "ds_read_b64_tr_b16 v[172:173], v197 offset:4096\n"
    "v_mfma_f32_16x16x32_bf16 v[36:39], v[132:135], v[116:119], v[36:39]\n"
    "ds_read_b64_tr_b16 v[174:175], v197 offset:4608\n"
    "v_mfma_f32_16x16x32_bf16 v[40:43], v[132:135], v[120:123], v[40:43]\n"
    "ds_read_b128 v[160:163], v195 offset:4096\n"
    "v_mfma_f32_16x16x32_bf16 v[44:47], v[132:135], v[124:127], v[44:47]\n"
    "ds_read_b64_tr_b16 v[176:177], v196 offset:12288\n"
    "v_mfma_f32_16x16x32_bf16 v[48:51], v[136:139], v[112:115], v[48:51]\n"
    "ds_read_b64_tr_b16 v[178:179], v196 offset:12800\n"
    "v_mfma_f32_16x16x32_bf16 v[52:55], v[136:139], v[116:119], v[52:55]\n"
    "ds_read_b128 v[164:167], v195 offset:6144\n"
    "v_mfma_f32_16x16x32_bf16 v[56:59], v[136:139], v[120:123], v[56:59]\n"
    "ds_read_b64_tr_b16 v[180:181], v197 offset:12288\n"
    "v_mfma_f32_16x16x32_bf16 v[60:63], v[136:139], v[124:127], v[60:63]\n"
    "ds_read_b64_tr_b16 v[182:183], v197 offset:12800\n"
    "v_mfma_f32_16x16x32_bf16 v[64:67], v[140:143], v[112:115], v[64:67]\n"
    "ds_read_b64_tr_b16 v[184:185], v196 offset:20480\n"
    "v_mfma_f32_16x16x32_bf16 v[68:71], v[140:143], v[116:119], v[68:71]\n"
    "ds_read_b64_tr_b16 v[186:187], v196 offset:20992\n"
    "v_mfma_f32_16x16x32_bf16 v[72:75], v[140:143], v[120:123], v[72:75]\n"
    "ds_read_b64_tr_b16 v[188:189], v197 offset:20480\n"
    "v_mfma_f32_16x16x32_bf16 v[76:79], v[140:143], v[124:127], v[76:79]\n"
    "ds_read_b64_tr_b16 v[190:191], v197 offset:20992\n"
    "v_mfma_f32_16x16x32_bf16 v[80:83], v[112:115], v[144:147], v[80:83]\n"
    "v_mfma_f32_16x16x32_bf16 v[84:87], v[116:119], v[144:147], v[84:87]\n"
    "v_mfma_f32_16x16x32_bf16 v[88:91], v[120:123], v[144:147], v[88:91]\n"
    "v_mfma_f32_16x16x32_bf16 v[92:95], v[124:127], v[144:147], v[92:95]\n"
    "v_mfma_f32_16x16x32_bf16 v[96:99], v[112:115], v[148:151], v[96:99]\n"
    "v_mfma_f32_16x16x32_bf16 v[100:103], v[116:119], v[148:151], v[100:103]\n"
    "v_mfma_f32_16x16x32_bf16 v[104:107], v[120:123], v[148:151], v[104:107]\n"
    "v_mfma_f32_16x16x32_bf16 v[108:111], v[124:127], v[148:151], v[108:111]\n"
    "s_cmp_eq_u32 %23, 7\n"
    "s_cbranch_scc1 Lt1_%=\n"
    "s_waitcnt vmcnt(0) lgkmcnt(0)\n"
    "s_barrier\n"
    "v_mfma_f32_16x16x32_bf16 v[16:19], v[168:171], v[152:155], v[16:19]\n"
    "ds_read_b128 v[112:115], %6 offset:0\n"
    "v_mfma_f32_16x16x32_bf16 v[20:23], v[168:171], v[156:159], v[20:23]\n"
    "ds_read_b64_tr_b16 v[128:129], %8 offset:0\n"
    "v_mfma_f32_16x16x32_bf16 v[24:27], v[168:171], v[160:163], v[24:27]\n"
    "ds_read_b64_tr_b16 v[130:131], %8 offset:512\n"
    "v_mfma_f32_16x16x32_bf16 v[28:31], v[168:171], v[164:167], v[28:31]\n"
    "ds_read_b128 v[116:119], %6 offset:2048\n"
    "v_mfma_f32_16x16x32_bf16 v[32:35], v[172:175], v[152:155], v[32:35]\n"
    "ds_read_b64_tr_b16 v[132:133], %9 offset:0\n"
    "s_add_u32 s40, s40, %22\n"
    "v_mfma_f32_16x16x32_bf16 v[36:39], v[172:175], v[156:159], v[36:39]\n"
    "ds_read_b64_tr_b16 v[134:135], %9 offset:512\n"
    "s_add_u32 m0, %21, 65536\n"
    "v_mfma_f32_16x16x32_bf16 v[40:43], v[172:175], v[160:163], v[40:43]\n"
    "ds_read_b128 v[120:123], %6 offset:4096\n"
    "s_nop 0\n"
    "v_mfma_f32_16x16x32_bf16 v[44:47], v[172:175], v[164:167], v[44:47]\n"
    "ds_read_b64_tr_b16 v[136:137], %8 offset:8192\n"
    "buffer_load_dwordx4 %10, %18, s40 offen offset:0 lds\n"
    "v_mfma_f32_16x16x32_bf16 v[48:51], v[176:179], v[152:155], v[48:51]\n"
    "ds_read_b64_tr_b16 v[138:139], %8 offset:8704\n"
    "buffer_load_dwordx4 %11, %18, s40 offen offset:1024 lds\n"
    "v_mfma_f32_16x16x32_bf16 v[52:55], v[176:179], v[156:159], v[52:55]\n"
    "ds_read_b128 v[124:127], %6 offset:6144\n"
    "buffer_load_dwordx4 %12, %18, s40 offen offset:2048 lds\n"
    "v_mfma_f32_16x16x32_bf16 v[56:59], v[176:179], v[160:163], v[56:59]\n"
    "ds_read_b64_tr_b16 v[140:141], %9 offset:8192\n"
    "buffer_load_dwordx4 %13, %18, s40 offen offset:3072 lds\n"
    "v_mfma_f32_16x16x32_bf16 v[60:63], v[176:179], v[164:167], v[60:63]\n"
    "ds_read_b64_tr_b16 v[142:143], %9 offset:8704\n"
    "s_add_u32 m0, %21, 69632\n"
    "v_mfma_f32_16x16x32_bf16 v[64:67], v[180:183], v[152:155], v[64:67]\n"
    "ds_read_b64_tr_b16 v[144:145], %8 offset:16384\n"
    "s_nop 0\n"
    "v_mfma_f32_16x16x32_bf16 v[68:71], v[180:183], v[156:159], v[68:71]\n"
    "ds_read_b64_tr_b16 v[146:147], %8 offset:16896\n"
    "buffer_load_dwordx4 %14, %18, s40 offen offset:0 lds\n"
    "v_mfma_f32_16x16x32_bf16 v[72:75], v[180:183], v[160:163], v[72:75]\n"
    "ds_read_b64_tr_b16 v[148:149], %9 offset:16384\n"
    "buffer_load_dwordx4 %15, %18, s40 offen offset:1024 lds\n"
    "v_mfma_f32_16x16x32_bf16 v[76:79], v[180:183], v[164:167], v[76:79]\n"
    "ds_read_b64_tr_b16 v[150:151], %9 offset:16896\n"
    "buffer_load_dwordx4 %16, %18, s40 offen offset:2048 lds\n"
    "v_mfma_f32_16x16x32_bf16 v[80:83], v[152:155], v[184:187], v[80:83]\n"
    "buffer_load_dwordx4 %17, %18, s40 offen offset:3072 lds\n"
    "v_mfma_f32_16x16x32_bf16 v[84:87], v[156:159], v[184:187], v[84:87]\n"
    "v_mfma_f32_16x16x32_bf16 v[88:91], v[160:163], v[184:187], v[88:91]\n"
    "v_mfma_f32_16x16x32_bf16 v[92:95], v[164:167], v[184:187], v[92:95]\n"
    "v_mfma_f32_16x16x32_bf16 v[96:99], v[152:155], v[188:191], v[96:99]\n"
    "v_mfma_f32_16x16x32_bf16 v[100:103], v[156:159], v[188:191], v[100:103]\n"
    "v_mfma_f32_16x16x32_bf16 v[104:107], v[160:163], v[188:191], v[104:107]\n"
    "v_mfma_f32_16x16x32_bf16 v[108:111], v[164:167], v[188:191], v[108:111]\n"
    "s_branch Lj1_%=\n"
    "Lt1_%=:\n"
    "s_waitcnt lgkmcnt(0)\n"
    "s_barrier\n"
    "v_mfma_f32_16x16x32_bf16 v[16:19], v[168:171], v[152:155], v[16:19]\n"
    "ds_read_b128 v[112:115], %6 offset:0\n"
    "v_mfma_f32_16x16x32_bf16 v[20:23], v[168:171], v[156:159], v[20:23]\n"
    "ds_read_b64_tr_b16 v[128:129], %8 offset:0\n"
    "v_mfma_f32_16x16x32_bf16 v[24:27], v[168:171], v[160:163], v[24:27]\n"
    "ds_read_b64_tr_b16 v[130:131], %8 offset:512\n"
    "v_mfma_f32_16x16x32_bf16 v[28:31], v[168:171], v[164:167], v[28:31]\n"
    "ds_read_b128 v[116:119], %6 offset:2048\n"
    "v_mfma_f32_16x16x32_bf16 v[32:35], v[172:175], v[152:155], v[32:35]\n"
    "ds_read_b64_tr_b16 v[132:133], %9 offset:0\n"
    "s_add_u32 s52, s52, 393216\n"
    "v_mfma_f32_16x16x32_bf16 v[36:39], v[172:175], v[156:159], v[36:39]\n"
    "ds_read_b64_tr_b16 v[134:135], %9 offset:512\n"
    "s_min_u32 s52, s52, 5898240\n"
    "v_mfma_f32_16x16x32_bf16 v[40:43], v[172:175], v[160:163], v[40:43]\n"
    "ds_read_b128 v[120:123], %6 offset:4096\n"
    "buffer_load_dword v193, %11, %20, s52 offen\n"
    "v_mfma_f32_16x16x32_bf16 v[44:47], v[172:175], v[164:167], v[44:47]\n"
    "ds_read_b64_tr_b16 v[136:137], %8 offset:8192\n"
    "v_mfma_f32_16x16x32_bf16 v[48:51], v[176:179], v[152:155], v[48:51]\n"
    "ds_read_b64_tr_b16 v[138:139], %8 offset:8704\n"
    "v_mfma_f32_16x16x32_bf16 v[52:55], v[176:179], v[156:159], v[52:55]\n"
    "ds_read_b128 v[124:127], %6 offset:6144\n"
    "v_mfma_f32_16x16x32_bf16 v[56:59], v[176:179], v[160:163], v[56:59]\n"
    "ds_read_b64_tr_b16 v[140:141], %9 offset:8192\n"
    "v_mfma_f32_16x16x32_bf16 v[60:63], v[176:179], v[164:167], v[60:63]\n"
    "ds_read_b64_tr_b16 v[142:143], %9 offset:8704\n"
    "v_mfma_f32_16x16x32_bf16 v[64:67], v[180:183], v[152:155], v[64:67]\n"
    "ds_read_b64_tr_b16 v[144:145], %8 offset:16384\n"
    "v_mfma_f32_16x16x32_bf16 v[68:71], v[180:183], v[156:159], v[68:71]\n"
    "ds_read_b64_tr_b16 v[146:147], %8 offset:16896\n"
    "v_mfma_f32_16x16x32_bf16 v[72:75], v[180:183], v[160:163], v[72:75]\n"
    "ds_read_b64_tr_b16 v[148:149], %9 offset:16384\n"
    "v_mfma_f32_16x16x32_bf16 v[76:79], v[180:183], v[164:167], v[76:79]\n"
    "ds_read_b64_tr_b16 v[150:151], %9 offset:16896\n"
    "v_mfma_f32_16x16x32_bf16 v[80:83], v[152:155], v[184:187], v[80:83]\n"
    "v_mfma_f32_16x16x32_bf16 v[84:87], v[156:159], v[184:187], v[84:87]\n"
    "v_mfma_f32_16x16x32_bf16 v[88:91], v[160:163], v[184:187], v[88:91]\n"
    "v_mfma_f32_16x16x32_bf16 v[92:95], v[164:167], v[184:187], v[92:95]\n"
    "v_mfma_f32_16x16x32_bf16 v[96:99], v[152:155], v[188:191], v[96:99]\n"
    "v_mfma_f32_16x16x32_bf16 v[100:103], v[156:159], v[188:191], v[100:103]\n"
    "v_mfma_f32_16x16x32_bf16 v[104:107], v[160:163], v[188:191], v[104:107]\n"
    "v_mfma_f32_16x16x32_bf16 v[108:111], v[164:167], v[188:191], v[108:111]\n"
    "Lj1_%=:\n"
    "s_sub_u32 s42, s42, 1\n"
    "s_cbranch_scc0 Lqkv_loop_%=\n"
    "s_waitcnt lgkmcnt(0)\n"
    "v_mfma_f32_16x16x32_bf16 v[16:19], v[128:131], v[112:115], v[16:19]\n"
    "ds_read_b128 v[152:155], %7 offset:0\n"
    "v_mfma_f32_16x16x32_bf16 v[20:23], v[128:131], v[116:119], v[20:23]\n"
    "ds_read_b64_tr_b16 v[168:169], %8 offset:4096\n"
    "v_mfma_f32_16x16x32_bf16 v[24:27], v[128:131], v[120:123], v[24:27]\n"
    "ds_read_b64_tr_b16 v[170:171], %8 offset:4608\n"
    "v_mfma_f32_16x16x32_bf16 v[28:31], v[128:131], v[124:127], v[28:31]\n"
    "ds_read_b128 v[156:159], %7 offset:2048\n"
    "v_mfma_f32_16x16x32_bf16 v[32:35], v[132:135], v[112:115], v[32:35]\n"
    "ds_read_b64_tr_b16 v[172:173], %9 offset:4096\n"
    "v_mfma_f32_16x16x32_bf16 v[36:39], v[132:135], v[116:119], v[36:39]\n"
    "ds_read_b64_tr_b16 v[174:175], %9 offset:4608\n"
    "v_mfma_f32_16x16x32_bf16 v[40:43], v[132:135], v[120:123], v[40:43]\n"
    "ds_read_b128 v[160:163], %7 offset:4096\n"
    "v_mfma_f32_16x16x32_bf16 v[44:47], v[132:135], v[124:127], v[44:47]\n"
    "ds_read_b64_tr_b16 v[176:177], %8 offset:12288\n"
    "v_mfma_f32_16x16x32_bf16 v[48:51], v[136:139], v[112:115], v[48:51]\n"
    "ds_read_b64_tr_b16 v[178:179], %8 offset:12800\n"
    "v_mfma_f32_16x16x32_bf16 v[52:55], v[136:139], v[116:119], v[52:55]\n"
    "ds_read_b128 v[164:167], %7 offset:6144\n"
    "v_mfma_f32_16x16x32_bf16 v[56:59], v[136:139], v[120:123], v[56:59]\n"
    "ds_read_b64_tr_b16 v[180:181], %9 offset:12288\n"
    "v_mfma_f32_16x16x32_bf16 v[60:63], v[136:139], v[124:127], v[60:63]\n"
    "ds_read_b64_tr_b16 v[182:183], %9 offset:12800\n"
    "v_mfma_f32_16x16x32_bf16 v[64:67], v[140:143], v[112:115], v[64:67]\n"
    "ds_read_b64_tr_b16 v[184:185], %8 offset:20480\n"
    "v_mfma_f32_16x16x32_bf16 v[68:71], v[140:143], v[116:119], v[68:71]\n"
    "ds_read_b64_tr_b16 v[186:187], %8 offset:20992\n"
    "v_mfma_f32_16x16x32_bf16 v[72:75], v[140:143], v[120:123], v[72:75]\n"
    "ds_read_b64_tr_b16 v[188:189], %9 offset:20480\n"
    "v_mfma_f32_16x16x32_bf16 v[76:79], v[140:143], v[124:127], v[76:79]\n"
    "ds_read_b64_tr_b16 v[190:191], %9 offset:20992\n"
    "v_mfma_f32_16x16x32_bf16 v[80:83], v[112:115], v[144:147], v[80:83]\n"
    "v_mfma_f32_16x16x32_bf16 v[84:87], v[116:119], v[144:147], v[84:87]\n"
    "v_mfma_f32_16x16x32_bf16 v[88:91], v[120:123], v[144:147], v[88:91]\n"
    "v_mfma_f32_16x16x32_bf16 v[92:95], v[124:127], v[144:147], v[92:95]\n"
    "v_mfma_f32_16x16x32_bf16 v[96:99], v[112:115], v[148:151], v[96:99]\n"
    "v_mfma_f32_16x16x32_bf16 v[100:103], v[116:119], v[148:151], v[100:103]\n"
    "v_mfma_f32_16x16x32_bf16 v[104:107], v[120:123], v[148:151], v[104:107]\n"
    "v_mfma_f32_16x16x32_bf16 v[108:111], v[124:127], v[148:151], v[108:111]\n"
    "s_cmp_eq_u32 %23, 7\n"
    "s_cbranch_scc1 Lnw2_%=\n"
    "s_waitcnt vmcnt(0)\n"
    "Lnw2_%=:\n"
    "s_waitcnt lgkmcnt(0)\n"
    "s_barrier\n"
    "v_mfma_f32_16x16x32_bf16 v[16:19], v[168:171], v[152:155], v[16:19]\n"
    "ds_read_b128 v[112:115], v194 offset:0\n"
    "v_mfma_f32_16x16x32_bf16 v[20:23], v[168:171], v[156:159], v[20:23]\n"
    "ds_read_b64_tr_b16 v[128:129], v196 offset:0\n"
    "v_mfma_f32_16x16x32_bf16 v[24:27], v[168:171], v[160:163], v[24:27]\n"
    "ds_read_b64_tr_b16 v[130:131], v196 offset:512\n"
    "v_mfma_f32_16x16x32_bf16 v[28:31], v[168:171], v[164:167], v[28:31]\n"
    "ds_read_b128 v[116:119], v194 offset:2048\n"
    "v_mfma_f32_16x16x32_bf16 v[32:35], v[172:175], v[152:155], v[32:35]\n"
    "ds_read_b64_tr_b16 v[132:133], v197 offset:0\n"
    "v_mfma_f32_16x16x32_bf16 v[36:39], v[172:175], v[156:159], v[36:39]\n"
    "ds_read_b64_tr_b16 v[134:135], v197 offset:512\n"
    "v_mfma_f32_16x16x32_bf16 v[40:43], v[172:175], v[160:163], v[40:43]\n"
    "ds_read_b128 v[120:123], v194 offset:4096\n"
    "v_mfma_f32_16x16x32_bf16 v[44:47], v[172:175], v[164:167], v[44:47]\n"
    "ds_read_b64_tr_b16 v[136:137], v196 offset:8192\n"
    "v_mfma_f32_16x16x32_bf16 v[48:51], v[176:179], v[152:155], v[48:51]\n"
    "ds_read_b64_tr_b16 v[138:139], v196 offset:8704\n"
    "v_mfma_f32_16x16x32_bf16 v[52:55], v[176:179], v[156:159], v[52:55]\n"
    "ds_read_b128 v[124:127], v194 offset:6144\n"
    "v_mfma_f32_16x16x32_bf16 v[56:59], v[176:179], v[160:163], v[56:59]\n"
    "ds_read_b64_tr_b16 v[140:141], v197 offset:8192\n"
    "v_mfma_f32_16x16x32_bf16 v[60:63], v[176:179], v[164:167], v[60:63]\n"
    "ds_read_b64_tr_b16 v[142:143], v197 offset:8704\n"
    "v_mfma_f32_16x16x32_bf16 v[64:67], v[180:183], v[152:155], v[64:67]\n"
    "ds_read_b64_tr_b16 v[144:145], v196 offset:16384\n"
    "v_mfma_f32_16x16x32_bf16 v[68:71], v[180:183], v[156:159], v[68:71]\n"
    "ds_read_b64_tr_b16 v[146:147], v196 offset:16896\n"
    "v_mfma_f32_16x16x32_bf16 v[72:75], v[180:183], v[160:163], v[72:75]\n"
    "ds_read_b64_tr_b16 v[148:149], v197 offset:16384\n"
    "v_mfma_f32_16x16x32_bf16 v[76:79], v[180:183], v[164:167], v[76:79]\n"
    "ds_read_b64_tr_b16 v[150:151], v197 offset:16896\n"
    "v_mfma_f32_16x16x32_bf16 v[80:83], v[152:155], v[184:187], v[80:83]\n"
    "v_mfma_f32_16x16x32_bf16 v[84:87], v[156:159], v[184:187], v[84:87]\n"
    "v_mfma_f32_16x16x32_bf16 v[88:91], v[160:163], v[184:187], v[88:91]\n"
    "v_mfma_f32_16x16x32_bf16 v[92:95], v[164:167], v[184:187], v[92:95]\n"
    "v_mfma_f32_16x16x32_bf16 v[96:99], v[152:155], v[188:191], v[96:99]\n"
    "v_mfma_f32_16x16x32_bf16 v[100:103], v[156:159], v[188:191], v[100:103]\n"
    "v_mfma_f32_16x16x32_bf16 v[104:107], v[160:163], v[188:191], v[104:107]\n"
    "v_mfma_f32_16x16x32_bf16 v[108:111], v[164:167], v[188:191], v[108:111]\n"
    "s_waitcnt lgkmcnt(0)\n"
    "v_mfma_f32_16x16x32_bf16 v[16:19], v[128:131], v[112:115], v[16:19]\n"
    "ds_read_b128 v[152:155], v195 offset:0\n"
    "v_mfma_f32_16x16x32_bf16 v[20:23], v[128:131], v[116:119], v[20:23]\n"
    "ds_read_b64_tr_b16 v[168:169], v196 offset:4096\n"
    "v_mfma_f32_16x16x32_bf16 v[24:27], v[128:131], v[120:123], v[24:27]\n"
    "ds_read_b64_tr_b16 v[170:171], v196 offset:4608\n"
    "v_mfma_f32_16x16x32_bf16 v[28:31], v[128:131], v[124:127], v[28:31]\n"
    "ds_read_b128 v[156:159], v195 offset:2048\n"
    "v_mfma_f32_16x16x32_bf16 v[32:35], v[132:135], v[112:115], v[32:35]\n"
    "ds_read_b64_tr_b16 v[172:173], v197 offset:4096\n"
    "v_mfma_f32_16x16x32_bf16 v[36:39], v[132:135], v[116:119], v[36:39]\n"
    "ds_read_b64_tr_b16 v[174:175], v197 offset:4608\n"
    "v_mfma_f32_16x16x32_bf16 v[40:43], v[132:135], v[120:123], v[40:43]\n"
    "ds_read_b128 v[160:163], v195 offset:4096\n"
    "v_mfma_f32_16x16x32_bf16 v[44:47], v[132:135], v[124:127], v[44:47]\n"
    "ds_read_b64_tr_b16 v[176:177], v196 offset:12288\n"
    "v_mfma_f32_16x16x32_bf16 v[48:51], v[136:139], v[112:115], v[48:51]\n"
    "ds_read_b64_tr_b16 v[178:179], v196 offset:12800\n"
    "v_mfma_f32_16x16x32_bf16 v[52:55], v[136:139], v[116:119], v[52:55]\n"
    "ds_read_b128 v[164:167], v195 offset:6144\n"
    "v_mfma_f32_16x16x32_bf16 v[56:59], v[136:139], v[120:123], v[56:59]\n"
    "ds_read_b64_tr_b16 v[180:181], v197 offset:12288\n"
    "v_mfma_f32_16x16x32_bf16 v[60:63], v[136:139], v[124:127], v[60:63]\n"
    "ds_read_b64_tr_b16 v[182:183], v197 offset:12800\n"
    "v_mfma_f32_16x16x32_bf16 v[64:67], v[140:143], v[112:115], v[64:67]\n"
    "ds_read_b64_tr_b16 v[184:185], v196 offset:20480\n"
    "v_mfma_f32_16x16x32_bf16 v[68:71], v[140:143], v[116:119], v[68:71]\n"
    "ds_read_b64_tr_b16 v[186:187], v196 offset:20992\n"
    "v_mfma_f32_16x16x32_bf16 v[72:75], v[140:143], v[120:123], v[72:75]\n"
    "ds_read_b64_tr_b16 v[188:189], v197 offset:20480\n"
    "v_mfma_f32_16x16x32_bf16 v[76:79], v[140:143], v[124:127], v[76:79]\n"
    "ds_read_b64_tr_b16 v[190:191], v197 offset:20992\n"
    "v_mfma_f32_16x16x32_bf16 v[80:83], v[112:115], v[144:147], v[80:83]\n"
    "v_mfma_f32_16x16x32_bf16 v[84:87], v[116:119], v[144:147], v[84:87]\n"
    "v_mfma_f32_16x16x32_bf16 v[88:91], v[120:123], v[144:147], v[88:91]\n"
    "v_mfma_f32_16x16x32_bf16 v[92:95], v[124:127], v[144:147], v[92:95]\n"
    "v_mfma_f32_16x16x32_bf16 v[96:99], v[112:115], v[148:151], v[96:99]\n"
    "v_mfma_f32_16x16x32_bf16 v[100:103], v[116:119], v[148:151], v[100:103]\n"
    "v_mfma_f32_16x16x32_bf16 v[104:107], v[120:123], v[148:151], v[104:107]\n"
    "v_mfma_f32_16x16x32_bf16 v[108:111], v[124:127], v[148:151], v[108:111]\n"
    "s_waitcnt lgkmcnt(0)\n"
    "v_mfma_f32_16x16x32_bf16 v[16:19], v[168:171], v[152:155], v[16:19]\n"
    "v_mfma_f32_16x16x32_bf16 v[20:23], v[168:171], v[156:159], v[20:23]\n"
    "v_mfma_f32_16x16x32_bf16 v[24:27], v[168:171], v[160:163], v[24:27]\n"
    "v_mfma_f32_16x16x32_bf16 v[28:31], v[168:171], v[164:167], v[28:31]\n"
    "v_mfma_f32_16x16x32_bf16 v[32:35], v[172:175], v[152:155], v[32:35]\n"
    "v_mfma_f32_16x16x32_bf16 v[36:39], v[172:175], v[156:159], v[36:39]\n"
    "v_mfma_f32_16x16x32_bf16 v[40:43], v[172:175], v[160:163], v[40:43]\n"
    "v_mfma_f32_16x16x32_bf16 v[44:47], v[172:175], v[164:167], v[44:47]\n"
    "v_mfma_f32_16x16x32_bf16 v[48:51], v[176:179], v[152:155], v[48:51]\n"
    "v_mfma_f32_16x16x32_bf16 v[52:55], v[176:179], v[156:159], v[52:55]\n"
    "v_mfma_f32_16x16x32_bf16 v[56:59], v[176:179], v[160:163], v[56:59]\n"
    "v_mfma_f32_16x16x32_bf16 v[60:63], v[176:179], v[164:167], v[60:63]\n"
    "v_mfma_f32_16x16x32_bf16 v[64:67], v[180:183], v[152:155], v[64:67]\n"
    "v_mfma_f32_16x16x32_bf16 v[68:71], v[180:183], v[156:159], v[68:71]\n"
    "v_mfma_f32_16x16x32_bf16 v[72:75], v[180:183], v[160:163], v[72:75]\n"
    "v_mfma_f32_16x16x32_bf16 v[76:79], v[180:183], v[164:167], v[76:79]\n"
    "v_mfma_f32_16x16x32_bf16 v[80:83], v[152:155], v[184:187], v[80:83]\n"
    "v_mfma_f32_16x16x32_bf16 v[84:87], v[156:159], v[184:187], v[84:87]\n"
    "v_mfma_f32_16x16x32_bf16 v[88:91], v[160:163], v[184:187], v[88:91]\n"
    "v_mfma_f32_16x16x32_bf16 v[92:95], v[164:167], v[184:187], v[92:95]\n"
    "v_mfma_f32_16x16x32_bf16 v[96:99], v[152:155], v[188:191], v[96:99]\n"
    "v_mfma_f32_16x16x32_bf16 v[100:103], v[156:159], v[188:191], v[100:103]\n"
    "v_mfma_f32_16x16x32_bf16 v[104:107], v[160:163], v[188:191], v[104:107]\n"
    "v_mfma_f32_16x16x32_bf16 v[108:111], v[164:167], v[188:191], v[108:111]\n"
    "s_waitcnt vmcnt(0)\n"
    "s_nop 15\n"
        : "=&{v[16:31]}"(A0), "=&{v[32:47]}"(A1), "=&{v[48:63]}"(A2), "=&{v[64:79]}"(A3), "=&{v[80:95]}"(A4), "=&{v[96:111]}"(A5)
        : "v"(xoff[0]), "v"(xoff[1]), "v"(woff[0]), "v"(woff[1]), "v"(vo[0]), "v"(vo[1]), "v"(vo[2]), "v"(vo[3]), "v"(vo[4]), "v"(vo[5]), "v"(vo[6]), "v"(vo[7]),
          "s"(ldr), "s"(xrt), "s"(wrt), "s"(lbase), "s"(sst), "s"(wid)
        : "v112", "v113", "v114", "v115", "v116", "v117", "v118", "v119", "v120", "v121", "v122", "v123", "v124", "v125", "v126", "v127", "v128", "v129", "v130", "v131", "v132", "v133", "v134", "v135", "v136", "v137", "v138", "v139", "v140", "v141", "v142", "v143", "v144", "v145", "v146", "v147", "v148", "v149", "v150", "v151", "v152", "v153", "v154", "v155", "v156", "v157", "v158", "v159", "v160", "v161", "v162", "v163", "v164", "v165", "v166", "v167", "v168", "v169", "v170", "v171", "v172", "v173", "v174", "v175", "v176", "v177", "v178", "v179", "v180", "v181", "v182", "v183", "v184", "v185", "v186", "v187", "v188", "v189", "v190", "v191", "v192", "v193", "v194", "v195", "v196", "v197", "v198", "v199", "v200", "s40", "s41", "s42", "s43", "s44", "s45", "s46", "s47", "s48", "s49", "s50", "s51", "s52", "s53", "s54", "s55", "s56", "s57", "vcc", "scc", "memory");
    asm volatile("" : "+v"(tag));
    tag += 1u;
    const __amdgpu_buffer_rsrc_t kvw = __builtin_amdgcn_make_buffer_rsrc((void*)(ws + WS_KV + ((size_t)(b * 32 + h) * 64 + rb * 8) * 4096), (short)0, 8 * 4096, 0x00020000);
    const int sp = g4 >> 1, hp1 = g4 & 1;
    u32x4 kk[4], vv[4];
    const unsigned ko0 = wm * 8192 + sp * 1024 + (16 * (l15 >> 3) + (l15 & 7) + 32 * hp1) * 16;
    const unsigned vo0 = wm * 8192 + 2048 + sp * 1024 + (l15 + 32 * hp1) * 16;
#pragma unroll
    for (int tt = 0; tt < 4; ++tt) { kk[tt] = pack44(A2, A3, tt, 1.0f); __builtin_amdgcn_raw_buffer_store_b128(kk[tt], kvw, ko0 + (tt >> 1) * 4096 + (tt & 1) * 128, 0, AUX_SC1); }
#pragma unroll
    for (int u = 0; u < 4; ++u) { vv[u] = pack44t((u & 1) ? A5 : A4, 2 * (u >> 1), 2 * (u >> 1) + 1); __builtin_amdgcn_raw_buffer_store_b128(vv[u], kvw, vo0 + (u >> 1) * 4096 + (u & 1) * 256, 0, AUX_SC1); }
    LAS char* own = lds + (wm >> 1) * 32768 + wn * 16384;
    const unsigned kl0 = ko0 & 16383, vl0 = vo0 & 16383;
#pragma unroll
    for (int tt = 0; tt < 4; ++tt) *(LAS u32x4*)(own + kl0 + (tt >> 1) * 4096 + (tt & 1) * 128) = kk[tt];
#pragma unroll
    for (int u = 0; u < 4; ++u) *(LAS u32x4*)(own + vl0 + (u >> 1) * 4096 + (u & 1) * 256) = vv[u];
#pragma unroll
    for (int i = 0; i < 2; ++i) {
      const u32x4 t0 = pack44(A0, A1, 2 * i, QSCALE), t1 = pack44(A0, A1, 2 * i + 1, QSCALE);
      u32x4 d0, d1;
#pragma unroll
      for (int r = 0; r < 4; ++r) {
        const auto s16 = __builtin_amdgcn_permlane16_swap(t0[r], t1[r], false, false);
        const auto s32 = __builtin_amdgcn_permlane32_swap(s16[0], s16[1], false, false);
        d0[r] = s32[0]; d1[r] = s32[1];
      }
      qf[i][0] = __builtin_bit_cast(bf16x8, d0); qf[i][1] = __builtin_bit_cast(bf16x8, d1);
    }
    asm volatile("" : "+v"(o[0]), "+v"(o[1]), "+v"(l[0]), "+v"(l[1]));
    asm volatile("s_waitcnt lgkmcnt(0)\n\ts_barrier" ::: "memory");
  }
  const int team = b * 16 + hp;
  const char* kvh = ws + WS_KV + (size_t)(b * 32 + h) * 64 * 4096;
  {
    const __amdgpu_buffer_rsrc_t kvr = __builtin_amdgcn_make_buffer_rsrc((void*)(ws + WS_KV + (size_t)(b * 32 + 2 * hp + (wid >> 2)) * 64 * 4096), (short)0, 64 * 4096, 0x00020000);
    bf16x8 ones; { short v = (((lane >> 4) & 1) == (((lane & 15) >> 2) & 1)) ? (short)0x3f80 : (short)0; ones = (bf16x8){v, v, v, v, v, v, v, v}; }
    const unsigned lb = ldsb + wn * 16384 + lane * 16, goff = (wid & 3) * 4096 + lane * 16;
    const unsigned ldsw = __builtin_amdgcn_readfirstlane(ldsb + wid * 4096);
    const unsigned soff0 = ((2 * rb + 2) & 15) * 16384;
    GAS unsigned* fl = (GAS unsigned*)(ws + WS_FKV) + team * 64;
    if (wid >= 4) __builtin_amdgcn_s_setprio(1);
    GAS unsigned* myflag = fl + rb * 8 + wid; GAS unsigned* pollp = fl + lane;
    const __amdgpu_buffer_rsrc_t wor = __builtin_amdgcn_make_buffer_rsrc((void*)((b ? wob : woa) + (hp & 7) * 128), (short)0, 1024 * 2048 - (hp & 7) * 256, 0x00020000);
    const unsigned wov = lane < 32 ? (unsigned)((128 * rb + 16 * wid + (lane >> 1)) * 2048 + (lane & 1) * 128) : 0x80000000u;
    asm volatile(
    "s_nop 1\n"
    "v_add_u32 v240, 65536, %9\n"
    "ds_read_b128 v[128:131], %9 offset:0\n"
    "ds_read_b128 v[132:135], %9 offset:1024\n"
    "ds_read_b128 v[152:155], %9 offset:2048\n"
    "ds_read_b128 v[156:159], %9 offset:3072\n"
    "s_waitcnt vmcnt(0)\n"
    "global_store_dword %14, %16, off sc1\n"
    "v_mov_b32 v200, 0\n"
    "v_mov_b32 v201, 0\n"
    "v_mov_b32 v202, 0\n"
    "v_mov_b32 v203, 0\n"
    "v_mov_b32 v204, 0\n"
    "v_mov_b32 v205, 0\n"
    "v_mov_b32 v206, 0\n"
    "v_mov_b32 v207, 0\n"
    "s_waitcnt lgkmcnt(2)\n"
    "v_mfma_f32_32x32x16_bf16 v[160:175], v[128:131], %4, 0\n"
    "v_mfma_f32_32x32x16_bf16 v[176:191], v[128:131], %6, 0\n"
    "v_mfma_f32_32x32x16_bf16 v[160:175], v[132:135], %5, v[160:175]\n"
    "v_mfma_f32_32x32x16_bf16 v[176:191], v[132:135], %7, v[176:191]\n"
    "v_mbcnt_lo_u32_b32 v192, -1, 0\n"
    "v_mbcnt_hi_u32_b32 v192, -1, v192\n"
    "v_cmp_gt_u32 vcc, 32, v192\n"
    "s_nop 1\n"
    "v_cndmask_b32 v192, 0, 1.0, vcc\n"
    "s_nop 9\n"
    "v_max3_f32 v241, v160, v161, v162\n"
    "v_max3_f32 v242, v176, v177, v178\n"
    "v_max3_f32 v241, v241, v163, v164\n"
    "v_max3_f32 v242, v242, v179, v180\n"
    "v_max3_f32 v241, v241, v165, v166\n"
    "v_max3_f32 v242, v242, v181, v182\n"
    "v_max3_f32 v241, v241, v167, v168\n"
    "v_max3_f32 v242, v242, v183, v184\n"
    "v_max3_f32 v241, v241, v169, v170\n"
    "v_max3_f32 v242, v242, v185, v186\n"
    "v_max3_f32 v241, v241, v171, v172\n"
    "v_max3_f32 v242, v242, v187, v188\n"
    "v_max3_f32 v241, v241, v173, v174\n"
    "v_max3_f32 v242, v242, v189, v190\n"
    "v_max_f32 v241, v241, v175\n"
    "v_max_f32 v242, v242, v191\n"
    "v_mov_b32 v193, v241\n"
    "v_mov_b32 v194, v242\n"
    "s_nop 1\n"
    "v_permlane32_swap_b32 v193, v241\n"
    "v_permlane32_swap_b32 v194, v242\n"
    "s_nop 1\n"
    "v_max_f32 v241, v241, v193\n"
    "v_max_f32 v242, v242, v194\n"
    "v_sub_f32 v241, 0, v241\n"
    "v_sub_f32 v242, 0, v242\n"
    "s_nop 1\n"
    "v_mfma_f32_32x32x2_f32 v[208:223], v192, v241, 0\n"
    "v_mfma_f32_32x32x2_f32 v[224:239], v192, v242, 0\n"
    "s_nop 15\n"
    "s_nop 2\n"
    "v_mfma_f32_32x32x16_bf16 v[160:175], v[128:131], %4, v[208:223]\n"
    "v_mfma_f32_32x32x16_bf16 v[160:175], v[132:135], %5, v[160:175]\n"
    "s_waitcnt lgkmcnt(0)\n"
    "s_nop 11\n"
    "v_mfma_f32_32x32x16_bf16 v[176:191], v[128:131], %6, v[224:239]\n"
    "ds_read_b128 v[136:139], %9 offset:4096\n"
    "v_exp_f32 v160, v160\n"
    "v_exp_f32 v161, v161\n"
    "v_exp_f32 v162, v162\n"
    "ds_read_b128 v[140:143], %9 offset:5120\n"
    "v_mfma_f32_32x32x16_bf16 v[176:191], v[132:135], %7, v[176:191]\n"
    "v_exp_f32 v163, v163\n"
    "v_cvt_pk_bf16_f32 v192, v160, v161\n"
    "ds_read_b128 v[144:147], %9 offset:2048\n"
    "v_exp_f32 v164, v164\n"
    "v_exp_f32 v165, v165\n"
    "ds_read_b128 v[148:151], %9 offset:3072\n"
    "v_mfma_f32_32x32x16_bf16 %1, v[152:155], v[200:203], %1\n"
    "v_cvt_pk_bf16_f32 v193, v162, v163\n"
    "v_exp_f32 v166, v166\n"
    "v_exp_f32 v167, v167\n"
    "v_cvt_pk_bf16_f32 v194, v164, v165\n"
    "v_mfma_f32_16x16x32_bf16 %3, %8, v[200:203], %3\n"
    "v_exp_f32 v168, v168\n"
    "v_exp_f32 v169, v169\n"
    "v_mfma_f32_32x32x16_bf16 %1, v[156:159], v[204:207], %1\n"
    "v_cvt_pk_bf16_f32 v195, v166, v167\n"
    "v_exp_f32 v170, v170\n"
    "v_exp_f32 v171, v171\n"
    "v_cvt_pk_bf16_f32 v196, v168, v169\n"
    "v_mfma_f32_16x16x32_bf16 %3, %8, v[204:207], %3\n"
    "v_exp_f32 v172, v172\n"
    "v_exp_f32 v173, v173\n"
    "v_cvt_pk_bf16_f32 v197, v170, v171\n"
    "v_exp_f32 v174, v174\n"
    "v_exp_f32 v175, v175\n"
    "v_cvt_pk_bf16_f32 v198, v172, v173\n"
    "v_cvt_pk_bf16_f32 v199, v174, v175\n"
    "s_waitcnt lgkmcnt(0)\n"
    "v_mfma_f32_32x32x16_bf16 v[160:175], v[136:139], %4, v[208:223]\n"
    "v_exp_f32 v176, v176\n"
    "v_exp_f32 v177, v177\n"
    "v_exp_f32 v178, v178\n"
    "v_mfma_f32_32x32x16_bf16 v[160:175], v[140:143], %5, v[160:175]\n"
    "v_exp_f32 v179, v179\n"
    "v_cvt_pk_bf16_f32 v200, v176, v177\n"
    "v_exp_f32 v180, v180\n"
    "v_exp_f32 v181, v181\n"
    "v_mfma_f32_32x32x16_bf16 %0, v[144:147], v[192:195], %0\n"
    "v_cvt_pk_bf16_f32 v201, v178, v179\n"
    "v_exp_f32 v182, v182\n"
    "v_exp_f32 v183, v183\n"
    "v_cvt_pk_bf16_f32 v202, v180, v181\n"
    "v_mfma_f32_16x16x32_bf16 %2, %8, v[192:195], %2\n"
    "v_exp_f32 v184, v184\n"
    "v_exp_f32 v185, v185\n"
    "v_mfma_f32_32x32x16_bf16 %0, v[148:151], v[196:199], %0\n"
    "v_cvt_pk_bf16_f32 v203, v182, v183\n"
    "v_exp_f32 v186, v186\n"
    "v_exp_f32 v187, v187\n"
    "v_cvt_pk_bf16_f32 v204, v184, v185\n"
    "v_mfma_f32_16x16x32_bf16 %2, %8, v[196:199], %2\n"
    "v_exp_f32 v188, v188\n"
    "v_exp_f32 v189, v189\n"
    "v_cvt_pk_bf16_f32 v205, v186, v187\n"
    "v_exp_f32 v190, v190\n"
    "v_exp_f32 v191, v191\n"
    "v_cvt_pk_bf16_f32 v206, v188, v189\n"
    "v_cvt_pk_bf16_f32 v207, v190, v191\n"
    "v_mfma_f32_32x32x16_bf16 v[176:191], v[136:139], %6, v[224:239]\n"
    "ds_read_b128 v[128:131], %9 offset:8192\n"
    "v_exp_f32 v160, v160\n"
    "v_exp_f32 v161, v161\n"
    "v_exp_f32 v162, v162\n"
    "ds_read_b128 v[132:135], %9 offset:9216\n"
    "v_mfma_f32_32x32x16_bf16 v[176:191], v[140:143], %7, v[176:191]\n"
    "v_exp_f32 v163, v163\n"
    "v_cvt_pk_bf16_f32 v192, v160, v161\n"
    "ds_read_b128 v[152:155], %9 offset:6144\n"
    "v_exp_f32 v164, v164\n"
    "v_exp_f32 v165, v165\n"
    "ds_read_b128 v[156:159], %9 offset:7168\n"
    "v_mfma_f32_32x32x16_bf16 %1, v[144:147], v[200:203], %1\n"
    "v_cvt_pk_bf16_f32 v193, v162, v163\n"
    "v_exp_f32 v166, v166\n"
    "v_exp_f32 v167, v167\n"
    "v_cvt_pk_bf16_f32 v194, v164, v165\n"
    "v_mfma_f32_16x16x32_bf16 %3, %8, v[200:203], %3\n"
    "v_exp_f32 v168, v168\n"
    "v_exp_f32 v169, v169\n"
    "v_mfma_f32_32x32x16_bf16 %1, v[148:151], v[204:207], %1\n"
    "v_cvt_pk_bf16_f32 v195, v166, v167\n"
    "v_exp_f32 v170, v170\n"
    "v_exp_f32 v171, v171\n"
    "v_cvt_pk_bf16_f32 v196, v168, v169\n"
    "v_mfma_f32_16x16x32_bf16 %3, %8, v[204:207], %3\n"
    "v_exp_f32 v172, v172\n"
    "v_exp_f32 v173, v173\n"
    "v_cvt_pk_bf16_f32 v197, v170, v171\n"
    "v_exp_f32 v174, v174\n"
    "v_exp_f32 v175, v175\n"
    "v_cvt_pk_bf16_f32 v198, v172, v173\n"
    "v_cvt_pk_bf16_f32 v199, v174, v175\n"
    "s_waitcnt lgkmcnt(0)\n"
    "v_mfma_f32_32x32x16_bf16 v[160:175], v[128:131], %4, v[208:223]\n"
    "v_exp_f32 v176, v176\n"
    "v_exp_f32 v177, v177\n"
    "v_exp_f32 v178, v178\n"
    "v_mfma_f32_32x32x16_bf16 v[160:175], v[132:135], %5, v[160:175]\n"
    "v_exp_f32 v179, v179\n"
    "v_cvt_pk_bf16_f32 v200, v176, v177\n"
    "v_exp_f32 v180, v180\n"
    "v_exp_f32 v181, v181\n"
    "v_mfma_f32_32x32x16_bf16 %0, v[152:155], v[192:195], %0\n"
    "v_cvt_pk_bf16_f32 v201, v178, v179\n"
    "v_exp_f32 v182, v182\n"
    "v_exp_f32 v183, v183\n"
    "v_cvt_pk_bf16_f32 v202, v180, v181\n"
    "v_mfma_f32_16x16x32_bf16 %2, %8, v[192:195], %2\n"
    "v_exp_f32 v184, v184\n"
    "v_exp_f32 v185, v185\n"
    "v_mfma_f32_32x32x16_bf16 %0, v[156:159], v[196:199], %0\n"
    "v_cvt_pk_bf16_f32 v203, v182, v183\n"
    "v_exp_f32 v186, v186\n"
    "v_exp_f32 v187, v187\n"
    "v_cvt_pk_bf16_f32 v204, v184, v185\n"
    "v_mfma_f32_16x16x32_bf16 %2, %8, v[196:199], %2\n"
    "v_exp_f32 v188, v188\n"
    "v_exp_f32 v189, v189\n"
    "v_cvt_pk_bf16_f32 v205, v186, v187\n"
    "v_exp_f32 v190, v190\n"
    "v_exp_f32 v191, v191\n"
    "v_cvt_pk_bf16_f32 v206, v188, v189\n"
    "v_cvt_pk_bf16_f32 v207, v190, v191\n"
    "v_mfma_f32_32x32x16_bf16 v[176:191], v[128:131], %6, v[224:239]\n"
    "ds_read_b128 v[136:139], %9 offset:12288\n"
    "v_exp_f32 v160, v160\n"
    "v_exp_f32 v161, v161\n"
    "v_exp_f32 v162, v162\n"
    "ds_read_b128 v[140:143], %9 offset:13312\n"
    "v_mfma_f32_32x32x16_bf16 v[176:191], v[132:135], %7, v[176:191]\n"
    "v_exp_f32 v163, v163\n"
    "v_cvt_pk_bf16_f32 v192, v160, v161\n"
    "ds_read_b128 v[144:147], %9 offset:10240\n"
    "v_exp_f32 v164, v164\n"
    "v_exp_f32 v165, v165\n"
    "ds_read_b128 v[148:151], %9 offset:11264\n"
    "v_mfma_f32_32x32x16_bf16 %1, v[152:155], v[200:203], %1\n"
    "v_cvt_pk_bf16_f32 v193, v162, v163\n"
    "v_exp_f32 v166, v166\n"
    "v_exp_f32 v167, v167\n"
    "v_cvt_pk_bf16_f32 v194, v164, v165\n"
    "v_mfma_f32_16x16x32_bf16 %3, %8, v[200:203], %3\n"
    "v_exp_f32 v168, v168\n"
    "v_exp_f32 v169, v169\n"
    "v_mfma_f32_32x32x16_bf16 %1, v[156:159], v[204:207], %1\n"
    "v_cvt_pk_bf16_f32 v195, v166, v167\n"
    "v_exp_f32 v170, v170\n"
    "v_exp_f32 v171, v171\n"
    "v_cvt_pk_bf16_f32 v196, v168, v169\n"
    "v_mfma_f32_16x16x32_bf16 %3, %8, v[204:207], %3\n"
    "v_exp_f32 v172, v172\n"
    "v_exp_f32 v173, v173\n"
    "v_cvt_pk_bf16_f32 v197, v170, v171\n"
    "v_exp_f32 v174, v174\n"
    "v_exp_f32 v175, v175\n"
    "v_cvt_pk_bf16_f32 v198, v172, v173\n"
    "v_cvt_pk_bf16_f32 v199, v174, v175\n"
    "s_waitcnt lgkmcnt(0)\n"
    "v_mfma_f32_32x32x16_bf16 v[160:175], v[136:139], %4, v[208:223]\n"
    "v_exp_f32 v176, v176\n"
    "v_exp_f32 v177, v177\n"
    "v_exp_f32 v178, v178\n"
    "v_mfma_f32_32x32x16_bf16 v[160:175], v[140:143], %5, v[160:175]\n"
    "v_exp_f32 v179, v179\n"
    "v_cvt_pk_bf16_f32 v200, v176, v177\n"
    "v_exp_f32 v180, v180\n"
    "v_exp_f32 v181, v181\n"
    "v_mfma_f32_32x32x16_bf16 %0, v[144:147], v[192:195], %0\n"
    "v_cvt_pk_bf16_f32 v201, v178, v179\n"
    "v_exp_f32 v182, v182\n"
    "v_exp_f32 v183, v183\n"
    "v_cvt_pk_bf16_f32 v202, v180, v181\n"
    "v_mfma_f32_16x16x32_bf16 %2, %8, v[192:195], %2\n"
    "v_exp_f32 v184, v184\n"
    "v_exp_f32 v185, v185\n"
    "v_mfma_f32_32x32x16_bf16 %0, v[148:151], v[196:199], %0\n"
    "v_cvt_pk_bf16_f32 v203, v182, v183\n"
    "v_exp_f32 v186, v186\n"
    "v_exp_f32 v187, v187\n"
    "v_cvt_pk_bf16_f32 v204, v184, v185\n"
    "v_mfma_f32_16x16x32_bf16 %2, %8, v[196:199], %2\n"
    "v_exp_f32 v188, v188\n"
    "v_exp_f32 v189, v189\n"
    "v_cvt_pk_bf16_f32 v205, v186, v187\n"
    "v_exp_f32 v190, v190\n"
    "v_exp_f32 v191, v191\n"
    "v_cvt_pk_bf16_f32 v206, v188, v189\n"
    "v_cvt_pk_bf16_f32 v207, v190, v191\n"
    "v_mfma_f32_32x32x16_bf16 v[176:191], v[136:139], %6, v[224:239]\n"
    "ds_read_b128 v[128:131], %9 offset:32768\n"
    "v_exp_f32 v160, v160\n"
    "v_exp_f32 v161, v161\n"
    "v_exp_f32 v162, v162\n"
    "ds_read_b128 v[132:135], %9 offset:33792\n"
    "v_mfma_f32_32x32x16_bf16 v[176:191], v[140:143], %7, v[176:191]\n"
    "v_exp_f32 v163, v163\n"
    "v_cvt_pk_bf16_f32 v192, v160, v161\n"
    "ds_read_b128 v[152:155], %9 offset:14336\n"
    "v_exp_f32 v164, v164\n"
    "v_exp_f32 v165, v165\n"
    "ds_read_b128 v[156:159], %9 offset:15360\n"
    "v_mfma_f32_32x32x16_bf16 %1, v[144:147], v[200:203], %1\n"
    "v_cvt_pk_bf16_f32 v193, v162, v163\n"
    "v_exp_f32 v166, v166\n"
    "v_exp_f32 v167, v167\n"
    "v_cvt_pk_bf16_f32 v194, v164, v165\n"
    "v_mfma_f32_16x16x32_bf16 %3, %8, v[200:203], %3\n"
    "v_exp_f32 v168, v168\n"
    "v_exp_f32 v169, v169\n"
    "v_mfma_f32_32x32x16_bf16 %1, v[148:151], v[204:207], %1\n"
    "v_cvt_pk_bf16_f32 v195, v166, v167\n"
    "v_exp_f32 v170, v170\n"
    "v_exp_f32 v171, v171\n"
    "v_cvt_pk_bf16_f32 v196, v168, v169\n"
    "v_mfma_f32_16x16x32_bf16 %3, %8, v[204:207], %3\n"
    "v_exp_f32 v172, v172\n"
    "v_exp_f32 v173, v173\n"
    "v_cvt_pk_bf16_f32 v197, v170, v171\n"
    "v_exp_f32 v174, v174\n"
    "v_exp_f32 v175, v175\n"
    "v_cvt_pk_bf16_f32 v198, v172, v173\n"
    "v_cvt_pk_bf16_f32 v199, v174, v175\n"
    "s_waitcnt lgkmcnt(0)\n"
    "v_mfma_f32_32x32x16_bf16 v[160:175], v[128:131], %4, v[208:223]\n"
    "v_exp_f32 v176, v176\n"
    "v_exp_f32 v177, v177\n"
    "v_exp_f32 v178, v178\n"
    "v_mfma_f32_32x32x16_bf16 v[160:175], v[132:135], %5, v[160:175]\n"
    "v_exp_f32 v179, v179\n"
    "v_cvt_pk_bf16_f32 v200, v176, v177\n"
    "v_exp_f32 v180, v180\n"
    "v_exp_f32 v181, v181\n"
    "v_mfma_f32_32x32x16_bf16 %0, v[152:155], v[192:195], %0\n"
    "v_cvt_pk_bf16_f32 v201, v178, v179\n"
    "v_exp_f32 v182, v182\n"
    "v_exp_f32 v183, v183\n"
    "v_cvt_pk_bf16_f32 v202, v180, v181\n"
    "v_mfma_f32_16x16x32_bf16 %2, %8, v[192:195], %2\n"
    "v_exp_f32 v184, v184\n"
    "v_exp_f32 v185, v185\n"
    "v_mfma_f32_32x32x16_bf16 %0, v[156:159], v[196:199], %0\n"
    "v_cvt_pk_bf16_f32 v203, v182, v183\n"
    "v_exp_f32 v186, v186\n"
    "v_exp_f32 v187, v187\n"
    "v_cvt_pk_bf16_f32 v204, v184, v185\n"
    "v_mfma_f32_16x16x32_bf16 %2, %8, v[196:199], %2\n"
    "v_exp_f32 v188, v188\n"
    "v_exp_f32 v189, v189\n"
    "v_cvt_pk_bf16_f32 v205, v186, v187\n"
    "v_exp_f32 v190, v190\n"
    "v_exp_f32 v191, v191\n"
    "v_cvt_pk_bf16_f32 v206, v188, v189\n"
    "v_cvt_pk_bf16_f32 v207, v190, v191\n"
    "v_mfma_f32_32x32x16_bf16 v[176:191], v[128:131], %6, v[224:239]\n"
    "ds_read_b128 v[136:139], %9 offset:36864\n"
    "v_exp_f32 v160, v160\n"
    "v_exp_f32 v161, v161\n"
    "v_exp_f32 v162, v162\n"
    "ds_read_b128 v[140:143], %9 offset:37888\n"
    "v_mfma_f32_32x32x16_bf16 v[176:191], v[132:135], %7, v[176:191]\n"
    "v_exp_f32 v163, v163\n"
    "v_cvt_pk_bf16_f32 v192, v160, v161\n"
    "ds_read_b128 v[144:147], %9 offset:34816\n"
    "v_exp_f32 v164, v164\n"
    "v_exp_f32 v165, v165\n"
    "ds_read_b128 v[148:151], %9 offset:35840\n"
    "v_mfma_f32_32x32x16_bf16 %1, v[152:155], v[200:203], %1\n"
    "v_cvt_pk_bf16_f32 v193, v162, v163\n"
    "v_exp_f32 v166, v166\n"
    "v_exp_f32 v167, v167\n"
    "v_cvt_pk_bf16_f32 v194, v164, v165\n"
    "v_mfma_f32_16x16x32_bf16 %3, %8, v[200:203], %3\n"
    "v_exp_f32 v168, v168\n"
    "v_exp_f32 v169, v169\n"
    "v_mfma_f32_32x32x16_bf16 %1, v[156:159], v[204:207], %1\n"
    "v_cvt_pk_bf16_f32 v195, v166, v167\n"
    "v_exp_f32 v170, v170\n"
    "v_exp_f32 v171, v171\n"
    "v_cvt_pk_bf16_f32 v196, v168, v169\n"
    "v_mfma_f32_16x16x32_bf16 %3, %8, v[204:207], %3\n"
    "v_exp_f32 v172, v172\n"
    "v_exp_f32 v173, v173\n"
    "v_cvt_pk_bf16_f32 v197, v170, v171\n"
    "v_exp_f32 v174, v174\n"
    "v_exp_f32 v175, v175\n"
    "v_cvt_pk_bf16_f32 v198, v172, v173\n"
    "v_cvt_pk_bf16_f32 v199, v174, v175\n"
    "s_waitcnt lgkmcnt(0)\n"
    "v_mfma_f32_32x32x16_bf16 v[160:175], v[136:139], %4, v[208:223]\n"
    "v_exp_f32 v176, v176\n"
    "v_exp_f32 v177, v177\n"
    "v_exp_f32 v178, v178\n"
    "v_mfma_f32_32x32x16_bf16 v[160:175], v[140:143], %5, v[160:175]\n"
    "v_exp_f32 v179, v179\n"
    "v_cvt_pk_bf16_f32 v200, v176, v177\n"
    "v_exp_f32 v180, v180\n"
    "v_exp_f32 v181, v181\n"
    "v_mfma_f32_32x32x16_bf16 %0, v[144:147], v[192:195], %0\n"
    "v_cvt_pk_bf16_f32 v201, v178, v179\n"
    "v_exp_f32 v182, v182\n"
    "v_exp_f32 v183, v183\n"
    "v_cvt_pk_bf16_f32 v202, v180, v181\n"
    "v_mfma_f32_16x16x32_bf16 %2, %8, v[192:195], %2\n"
    "v_exp_f32 v184, v184\n"
    "v_exp_f32 v185, v185\n"
    "v_mfma_f32_32x32x16_bf16 %0, v[148:151], v[196:199], %0\n"
    "v_cvt_pk_bf16_f32 v203, v182, v183\n"
    "v_exp_f32 v186, v186\n"
    "v_exp_f32 v187, v187\n"
    "v_cvt_pk_bf16_f32 v204, v184, v185\n"
    "v_mfma_f32_16x16x32_bf16 %2, %8, v[196:199], %2\n"
    "v_exp_f32 v188, v188\n"
    "v_exp_f32 v189, v189\n"
    "v_cvt_pk_bf16_f32 v205, v186, v187\n"
    "v_exp_f32 v190, v190\n"
    "v_exp_f32 v191, v191\n"
    "v_cvt_pk_bf16_f32 v206, v188, v189\n"
    "v_cvt_pk_bf16_f32 v207, v190, v191\n"
    "v_mfma_f32_32x32x16_bf16 v[176:191], v[136:139], %6, v[224:239]\n"
    "ds_read_b128 v[128:131], %9 offset:40960\n"
    "v_exp_f32 v160, v160\n"
    "v_exp_f32 v161, v161\n"
    "v_exp_f32 v162, v162\n"
    "ds_read_b128 v[132:135], %9 offset:41984\n"
    "v_mfma_f32_32x32x16_bf16 v[176:191], v[140:143], %7, v[176:191]\n"
    "v_exp_f32 v163, v163\n"
    "v_cvt_pk_bf16_f32 v192, v160, v161\n"
    "ds_read_b128 v[152:155], %9 offset:38912\n"
    "v_exp_f32 v164, v164\n"
    "v_exp_f32 v165, v165\n"
    "ds_read_b128 v[156:159], %9 offset:39936\n"
    "v_mfma_f32_32x32x16_bf16 %1, v[144:147], v[200:203], %1\n"
    "v_cvt_pk_bf16_f32 v193, v162, v163\n"
    "v_exp_f32 v166, v166\n"
    "v_exp_f32 v167, v167\n"
    "v_cvt_pk_bf16_f32 v194, v164, v165\n"
    "v_mfma_f32_16x16x32_bf16 %3, %8, v[200:203], %3\n"
    "v_exp_f32 v168, v168\n"
    "v_exp_f32 v169, v169\n"
    "v_mfma_f32_32x32x16_bf16 %1, v[148:151], v[204:207], %1\n"
    "v_cvt_pk_bf16_f32 v195, v166, v167\n"
    "v_exp_f32 v170, v170\n"
    "v_exp_f32 v171, v171\n"
    "v_cvt_pk_bf16_f32 v196, v168, v169\n"
    "v_mfma_f32_16x16x32_bf16 %3, %8, v[204:207], %3\n"
    "v_exp_f32 v172, v172\n"
    "v_exp_f32 v173, v173\n"
    "v_cvt_pk_bf16_f32 v197, v170, v171\n"
    "v_exp_f32 v174, v174\n"
    "v_exp_f32 v175, v175\n"
    "v_cvt_pk_bf16_f32 v198, v172, v173\n"
    "v_cvt_pk_bf16_f32 v199, v174, v175\n"
    "s_waitcnt lgkmcnt(0)\n"
    "s_mov_b32 s53, 0\n"
    "Lpoll_%=:\n"
    "global_load_dword v241, %15, off sc1\n"
    "s_waitcnt vmcnt(0)\n"
    "v_cmp_ne_u32 vcc, v241, %16\n"
    "s_nop 4\n"
    "s_cbranch_vccz Lready_%=\n"
    "s_sleep 2\n"
    "s_add_u32 s53, s53, 1\n"
    "s_cmp_lt_u32 s53, 200000\n"
    "s_cbranch_scc1 Lpoll_%=\n"
    "Lready_%=:\n"
    "s_mov_b32 s40, %13\n"
    "s_add_u32 m0, %12, 65536\n"
    "s_nop 0\n"
    "buffer_load_dwordx4 %10, %11, s40 offen offset:0 sc1 lds\n"
    "buffer_load_dwordx4 %10, %11, s40 offen offset:1024 sc1 lds\n"
    "buffer_load_dwordx4 %10, %11, s40 offen offset:2048 sc1 lds\n"
    "buffer_load_dwordx4 %10, %11, s40 offen offset:3072 sc1 lds\n"
    "s_add_u32 s40, s40, 16384\n"
    "s_and_b32 s40, s40, 0x3ffff\n"
    "s_add_u32 m0, %12, 98304\n"
    "s_nop 0\n"
    "buffer_load_dwordx4 %10, %11, s40 offen offset:0 sc1 lds\n"
    "buffer_load_dwordx4 %10, %11, s40 offen offset:1024 sc1 lds\n"
    "buffer_load_dwordx4 %10, %11, s40 offen offset:2048 sc1 lds\n"
    "buffer_load_dwordx4 %10, %11, s40 offen offset:3072 sc1 lds\n"
    "s_add_u32 s40, s40, 16384\n"
    "s_and_b32 s40, s40, 0x3ffff\n"
    "v_mfma_f32_32x32x16_bf16 v[160:175], v[128:131], %4, v[208:223]\n"
    "v_exp_f32 v176, v176\n"
    "v_exp_f32 v177, v177\n"
    "v_exp_f32 v178, v178\n"
    "v_mfma_f32_32x32x16_bf16 v[160:175], v[132:135], %5, v[160:175]\n"
    "v_exp_f32 v179, v179\n"
    "v_cvt_pk_bf16_f32 v200, v176, v177\n"
    "v_exp_f32 v180, v180\n"
    "v_exp_f32 v181, v181\n"
    "v_mfma_f32_32x32x16_bf16 %0, v[152:155], v[192:195], %0\n"
    "v_cvt_pk_bf16_f32 v201, v178, v179\n"
    "v_exp_f32 v182, v182\n"
    "v_exp_f32 v183, v183\n"
    "v_cvt_pk_bf16_f32 v202, v180, v181\n"
    "v_mfma_f32_16x16x32_bf16 %2, %8, v[192:195], %2\n"
    "v_exp_f32 v184, v184\n"
    "v_exp_f32 v185, v185\n"
    "v_mfma_f32_32x32x16_bf16 %0, v[156:159], v[196:199], %0\n"
    "v_cvt_pk_bf16_f32 v203, v182, v183\n"
    "v_exp_f32 v186, v186\n"
    "v_exp_f32 v187, v187\n"
    "v_cvt_pk_bf16_f32 v204, v184, v185\n"
    "v_mfma_f32_16x16x32_bf16 %2, %8, v[196:199], %2\n"
    "v_exp_f32 v188, v188\n"
    "v_exp_f32 v189, v189\n"
    "v_cvt_pk_bf16_f32 v205, v186, v187\n"
    "v_exp_f32 v190, v190\n"
    "v_exp_f32 v191, v191\n"
    "v_cvt_pk_bf16_f32 v206, v188, v189\n"
    "v_cvt_pk_bf16_f32 v207, v190, v191\n"
    "v_mfma_f32_32x32x16_bf16 v[176:191], v[128:131], %6, v[224:239]\n"
    "ds_read_b128 v[136:139], %9 offset:45056\n"
    "v_exp_f32 v160, v160\n"
    "v_exp_f32 v161, v161\n"
    "v_exp_f32 v162, v162\n"
    "ds_read_b128 v[140:143], %9 offset:46080\n"
    "v_mfma_f32_32x32x16_bf16 v[176:191], v[132:135], %7, v[176:191]\n"
    "v_exp_f32 v163, v163\n"
    "v_cvt_pk_bf16_f32 v192, v160, v161\n"
    "ds_read_b128 v[144:147], %9 offset:43008\n"
    "v_exp_f32 v164, v164\n"
    "v_exp_f32 v165, v165\n"
    "ds_read_b128 v[148:151], %9 offset:44032\n"
    "v_mfma_f32_32x32x16_bf16 %1, v[152:155], v[200:203], %1\n"
    "v_cvt_pk_bf16_f32 v193, v162, v163\n"
    "v_exp_f32 v166, v166\n"
    "v_exp_f32 v167, v167\n"
    "v_cvt_pk_bf16_f32 v194, v164, v165\n"
    "v_mfma_f32_16x16x32_bf16 %3, %8, v[200:203], %3\n"
    "v_exp_f32 v168, v168\n"
    "v_exp_f32 v169, v169\n"
    "v_mfma_f32_32x32x16_bf16 %1, v[156:159], v[204:207], %1\n"
    "v_cvt_pk_bf16_f32 v195, v166, v167\n"
    "v_exp_f32 v170, v170\n"
    "v_exp_f32 v171, v171\n"
    "v_cvt_pk_bf16_f32 v196, v168, v169\n"
    "v_mfma_f32_16x16x32_bf16 %3, %8, v[204:207], %3\n"
    "v_exp_f32 v172, v172\n"
    "v_exp_f32 v173, v173\n"
    "v_cvt_pk_bf16_f32 v197, v170, v171\n"
    "v_exp_f32 v174, v174\n"
    "v_exp_f32 v175, v175\n"
    "v_cvt_pk_bf16_f32 v198, v172, v173\n"
    "v_cvt_pk_bf16_f32 v199, v174, v175\n"
    "s_waitcnt lgkmcnt(0)\n"
    "v_mfma_f32_32x32x16_bf16 v[160:175], v[136:139], %4, v[208:223]\n"
    "v_exp_f32 v176, v176\n"
    "v_exp_f32 v177, v177\n"
    "v_exp_f32 v178, v178\n"
    "v_mfma_f32_32x32x16_bf16 v[160:175], v[140:143], %5, v[160:175]\n"
    "v_exp_f32 v179, v179\n"
    "v_cvt_pk_bf16_f32 v200, v176, v177\n"
    "v_exp_f32 v180, v180\n"
    "v_exp_f32 v181, v181\n"
    "v_mfma_f32_32x32x16_bf16 %0, v[144:147], v[192:195], %0\n"
    "v_cvt_pk_bf16_f32 v201, v178, v179\n"
    "v_exp_f32 v182, v182\n"
    "v_exp_f32 v183, v183\n"
    "v_cvt_pk_bf16_f32 v202, v180, v181\n"
    "v_mfma_f32_16x16x32_bf16 %2, %8, v[192:195], %2\n"
    "v_exp_f32 v184, v184\n"
    "v_exp_f32 v185, v185\n"
    "v_mfma_f32_32x32x16_bf16 %0, v[148:151], v[196:199], %0\n"
    "v_cvt_pk_bf16_f32 v203, v182, v183\n"
    "v_exp_f32 v186, v186\n"
    "v_exp_f32 v187, v187\n"
    "v_cvt_pk_bf16_f32 v204, v184, v185\n"
    "v_mfma_f32_16x16x32_bf16 %2, %8, v[196:199], %2\n"
    "v_exp_f32 v188, v188\n"
    "v_exp_f32 v189, v189\n"
    "v_cvt_pk_bf16_f32 v205, v186, v187\n"
    "v_exp_f32 v190, v190\n"
    "v_exp_f32 v191, v191\n"
    "v_cvt_pk_bf16_f32 v206, v188, v189\n"
    "v_cvt_pk_bf16_f32 v207, v190, v191\n"
    "s_waitcnt vmcnt(4)\n"
    "s_barrier\n"
    "v_mfma_f32_32x32x16_bf16 v[176:191], v[136:139], %6, v[224:239]\n"
    "ds_read_b128 v[128:131], v240 offset:0\n"
    "v_exp_f32 v160, v160\n"
    "v_exp_f32 v161, v161\n"
    "v_exp_f32 v162, v162\n"
    "ds_read_b128 v[132:135], v240 offset:1024\n"
    "v_mfma_f32_32x32x16_bf16 v[176:191], v[140:143], %7, v[176:191]\n"
    "v_exp_f32 v163, v163\n"
    "v_cvt_pk_bf16_f32 v192, v160, v161\n"
    "ds_read_b128 v[152:155], %9 offset:47104\n"
    "v_exp_f32 v164, v164\n"
    "v_exp_f32 v165, v165\n"
    "ds_read_b128 v[156:159], %9 offset:48128\n"
    "v_mfma_f32_32x32x16_bf16 %1, v[144:147], v[200:203], %1\n"
    "v_cvt_pk_bf16_f32 v193, v162, v163\n"
    "v_exp_f32 v166, v166\n"
    "v_exp_f32 v167, v167\n"
    "v_cvt_pk_bf16_f32 v194, v164, v165\n"
    "s_add_u32 m0, %12, 0\n"
    "v_mfma_f32_16x16x32_bf16 %3, %8, v[200:203], %3\n"
    "v_exp_f32 v168, v168\n"
    "v_exp_f32 v169, v169\n"
    "buffer_load_dwordx4 %10, %11, s40 offen offset:0 sc1 lds\n"
    "v_mfma_f32_32x32x16_bf16 %1, v[148:151], v[204:207], %1\n"
    "v_cvt_pk_bf16_f32 v195, v166, v167\n"
    "v_exp_f32 v170, v170\n"
    "v_exp_f32 v171, v171\n"
    "v_cvt_pk_bf16_f32 v196, v168, v169\n"
    "buffer_load_dwordx4 %10, %11, s40 offen offset:1024 sc1 lds\n"
    "v_mfma_f32_16x16x32_bf16 %3, %8, v[204:207], %3\n"
    "v_exp_f32 v172, v172\n"
    "v_exp_f32 v173, v173\n"
    "v_cvt_pk_bf16_f32 v197, v170, v171\n"
    "v_exp_f32 v174, v174\n"
    "v_exp_f32 v175, v175\n"
    "v_cvt_pk_bf16_f32 v198, v172, v173\n"
    "v_cvt_pk_bf16_f32 v199, v174, v175\n"
    "s_waitcnt lgkmcnt(0)\n"
    "v_mfma_f32_32x32x16_bf16 v[160:175], v[128:131], %4, v[208:223]\n"
    "buffer_load_dwordx4 %10, %11, s40 offen offset:2048 sc1 lds\n"
    "v_exp_f32 v176, v176\n"
    "v_exp_f32 v177, v177\n"
    "v_exp_f32 v178, v178\n"
    "buffer_load_dwordx4 %10, %11, s40 offen offset:3072 sc1 lds\n"
    "v_mfma_f32_32x32x16_bf16 v[160:175], v[132:135], %5, v[160:175]\n"
    "v_exp_f32 v179, v179\n"
    "v_cvt_pk_bf16_f32 v200, v176, v177\n"
    "s_add_u32 s40, s40, 16384\n"
    "v_exp_f32 v180, v180\n"
    "v_exp_f32 v181, v181\n"
    "s_and_b32 s40, s40, 0x3ffff\n"
    "v_mfma_f32_32x32x16_bf16 %0, v[152:155], v[192:195], %0\n"
    "v_cvt_pk_bf16_f32 v201, v178, v179\n"
    "v_exp_f32 v182, v182\n"
    "v_exp_f32 v183, v183\n"
    "v_cvt_pk_bf16_f32 v202, v180, v181\n"
    "v_mfma_f32_16x16x32_bf16 %2, %8, v[192:195], %2\n"
    "v_exp_f32 v184, v184\n"
    "v_exp_f32 v185, v185\n"
    "v_mfma_f32_32x32x16_bf16 %0, v[156:159], v[196:199], %0\n"
    "v_cvt_pk_bf16_f32 v203, v182, v183\n"
    "v_exp_f32 v186, v186\n"
    "v_exp_f32 v187, v187\n"
    "v_cvt_pk_bf16_f32 v204, v184, v185\n"
    "v_mfma_f32_16x16x32_bf16 %2, %8, v[196:199], %2\n"
    "v_exp_f32 v188, v188\n"
    "v_exp_f32 v189, v189\n"
    "v_cvt_pk_bf16_f32 v205, v186, v187\n"
    "v_exp_f32 v190, v190\n"
    "v_exp_f32 v191, v191\n"
    "v_cvt_pk_bf16_f32 v206, v188, v189\n"
    "v_cvt_pk_bf16_f32 v207, v190, v191\n"
    "s_mov_b32 s42, 2\n"
    "Lattn_loop_%=:\n"
    "v_mfma_f32_32x32x16_bf16 v[176:191], v[128:131], %6, v[224:239]\n"
    "ds_read_b128 v[136:139], v240 offset:4096\n"
    "v_exp_f32 v160, v160\n"
    "v_exp_f32 v161, v161\n"
    "v_exp_f32 v162, v162\n"
    "ds_read_b128 v[140:143], v240 offset:5120\n"
    "v_mfma_f32_32x32x16_bf16 v[176:191], v[132:135], %7, v[176:191]\n"
    "v_exp_f32 v163, v163\n"
    "v_cvt_pk_bf16_f32 v192, v160, v161\n"
    "ds_read_b128 v[144:147], v240 offset:2048\n"
    "v_exp_f32 v164, v164\n"
    "v_exp_f32 v165, v165\n"
    "ds_read_b128 v[148:151], v240 offset:3072\n"
    "v_mfma_f32_32x32x16_bf16 %1, v[152:155], v[200:203], %1\n"
    "v_cvt_pk_bf16_f32 v193, v162, v163\n"
    "v_exp_f32 v166, v166\n"
    "v_exp_f32 v167, v167\n"
    "v_cvt_pk_bf16_f32 v194, v164, v165\n"
    "v_mfma_f32_16x16x32_bf16 %3, %8, v[200:203], %3\n"
    "v_exp_f32 v168, v168\n"
    "v_exp_f32 v169, v169\n"
    "v_mfma_f32_32x32x16_bf16 %1, v[156:159], v[204:207], %1\n"
    "v_cvt_pk_bf16_f32 v195, v166, v167\n"
    "v_exp_f32 v170, v170\n"
    "v_exp_f32 v171, v171\n"
    "v_cvt_pk_bf16_f32 v196, v168, v169\n"
    "v_mfma_f32_16x16x32_bf16 %3, %8, v[204:207], %3\n"
    "v_exp_f32 v172, v172\n"
    "v_exp_f32 v173, v173\n"
    "v_cvt_pk_bf16_f32 v197, v170, v171\n"
    "v_exp_f32 v174, v174\n"
    "v_exp_f32 v175, v175\n"
    "v_cvt_pk_bf16_f32 v198, v172, v173\n"
    "v_cvt_pk_bf16_f32 v199, v174, v175\n"
    "s_waitcnt lgkmcnt(0)\n"
    "v_mfma_f32_32x32x16_bf16 v[160:175], v[136:139], %4, v[208:223]\n"
    "v_exp_f32 v176, v176\n"
    "v_exp_f32 v177, v177\n"
    "v_exp_f32 v178, v178\n"
    "v_mfma_f32_32x32x16_bf16 v[160:175], v[140:143], %5, v[160:175]\n"
    "v_exp_f32 v179, v179\n"
    "v_cvt_pk_bf16_f32 v200, v176, v177\n"
    "v_exp_f32 v180, v180\n"
    "v_exp_f32 v181, v181\n"
    "v_mfma_f32_32x32x16_bf16 %0, v[144:147], v[192:195], %0\n"
    "v_cvt_pk_bf16_f32 v201, v178, v179\n"
    "v_exp_f32 v182, v182\n"
    "v_exp_f32 v183, v183\n"
    "v_cvt_pk_bf16_f32 v202, v180, v181\n"
    "v_mfma_f32_16x16x32_bf16 %2, %8, v[192:195], %2\n"
    "v_exp_f32 v184, v184\n"
    "v_exp_f32 v185, v185\n"
    "v_mfma_f32_32x32x16_bf16 %0, v[148:151], v[196:199], %0\n"
    "v_cvt_pk_bf16_f32 v203, v182, v183\n"
    "v_exp_f32 v186, v186\n"
    "v_exp_f32 v187, v187\n"
    "v_cvt_pk_bf16_f32 v204, v184, v185\n"
    "v_mfma_f32_16x16x32_bf16 %2, %8, v[196:199], %2\n"
    "v_exp_f32 v188, v188\n"
    "v_exp_f32 v189, v189\n"
    "v_cvt_pk_bf16_f32 v205, v186, v187\n"
    "v_exp_f32 v190, v190\n"
    "v_exp_f32 v191, v191\n"
    "v_cvt_pk_bf16_f32 v206, v188, v189\n"
    "v_cvt_pk_bf16_f32 v207, v190, v191\n"
    "v_mfma_f32_32x32x16_bf16 v[176:191], v[136:139], %6, v[224:239]\n"
    "ds_read_b128 v[128:131], v240 offset:8192\n"
    "v_exp_f32 v160, v160\n"
    "v_exp_f32 v161, v161\n"
    "v_exp_f32 v162, v162\n"
    "ds_read_b128 v[132:135], v240 offset:9216\n"
    "v_mfma_f32_32x32x16_bf16 v[176:191], v[140:143], %7, v[176:191]\n"
    "v_exp_f32 v163, v163\n"
    "v_cvt_pk_bf16_f32 v192, v160, v161\n"
    "ds_read_b128 v[152:155], v240 offset:6144\n"
    "v_exp_f32 v164, v164\n"
    "v_exp_f32 v165, v165\n"
    "ds_read_b128 v[156:159], v240 offset:7168\n"
    "v_mfma_f32_32x32x16_bf16 %1, v[144:147], v[200:203], %1\n"
    "v_cvt_pk_bf16_f32 v193, v162, v163\n"
    "v_exp_f32 v166, v166\n"
    "v_exp_f32 v167, v167\n"
    "v_cvt_pk_bf16_f32 v194, v164, v165\n"
    "v_mfma_f32_16x16x32_bf16 %3, %8, v[200:203], %3\n"
    "v_exp_f32 v168, v168\n"
    "v_exp_f32 v169, v169\n"
    "v_mfma_f32_32x32x16_bf16 %1, v[148:151], v[204:207], %1\n"
    "v_cvt_pk_bf16_f32 v195, v166, v167\n"
    "v_exp_f32 v170, v170\n"
    "v_exp_f32 v171, v171\n"
    "v_cvt_pk_bf16_f32 v196, v168, v169\n"
    "v_mfma_f32_16x16x32_bf16 %3, %8, v[204:207], %3\n"
    "v_exp_f32 v172, v172\n"
    "v_exp_f32 v173, v173\n"
    "v_cvt_pk_bf16_f32 v197, v170, v171\n"
    "v_exp_f32 v174, v174\n"
    "v_exp_f32 v175, v175\n"
    "v_cvt_pk_bf16_f32 v198, v172, v173\n"
    "v_cvt_pk_bf16_f32 v199, v174, v175\n"
    "s_waitcnt lgkmcnt(0)\n"
    "v_mfma_f32_32x32x16_bf16 v[160:175], v[128:131], %4, v[208:223]\n"
    "v_exp_f32 v176, v176\n"
    "v_exp_f32 v177, v177\n"
    "v_exp_f32 v178, v178\n"
    "v_mfma_f32_32x32x16_bf16 v[160:175], v[132:135], %5, v[160:175]\n"
    "v_exp_f32 v179, v179\n"
    "v_cvt_pk_bf16_f32 v200, v176, v177\n"
    "v_exp_f32 v180, v180\n"
    "v_exp_f32 v181, v181\n"
    "v_mfma_f32_32x32x16_bf16 %0, v[152:155], v[192:195], %0\n"
    "v_cvt_pk_bf16_f32 v201, v178, v179\n"
    "v_exp_f32 v182, v182\n"
    "v_exp_f32 v183, v183\n"
    "v_cvt_pk_bf16_f32 v202, v180, v181\n"
    "v_mfma_f32_16x16x32_bf16 %2, %8, v[192:195], %2\n"
    "v_exp_f32 v184, v184\n"
    "v_exp_f32 v185, v185\n"
    "v_mfma_f32_32x32x16_bf16 %0, v[156:159], v[196:199], %0\n"
    "v_cvt_pk_bf16_f32 v203, v182, v183\n"
    "v_exp_f32 v186, v186\n"
    "v_exp_f32 v187, v187\n"
    "v_cvt_pk_bf16_f32 v204, v184, v185\n"
    "v_mfma_f32_16x16x32_bf16 %2, %8, v[196:199], %2\n"
    "v_exp_f32 v188, v188\n"
    "v_exp_f32 v189, v189\n"
    "v_cvt_pk_bf16_f32 v205, v186, v187\n"
    "v_exp_f32 v190, v190\n"
    "v_exp_f32 v191, v191\n"
    "v_cvt_pk_bf16_f32 v206, v188, v189\n"
    "v_cvt_pk_bf16_f32 v207, v190, v191\n"
    "v_mfma_f32_32x32x16_bf16 v[176:191], v[128:131], %6, v[224:239]\n"
    "ds_read_b128 v[136:139], v240 offset:12288\n"
    "v_exp_f32 v160, v160\n"
    "v_exp_f32 v161, v161\n"
    "v_exp_f32 v162, v162\n"
    "ds_read_b128 v[140:143], v240 offset:13312\n"
    "v_mfma_f32_32x32x16_bf16 v[176:191], v[132:135], %7, v[176:191]\n"
    "v_exp_f32 v163, v163\n"
    "v_cvt_pk_bf16_f32 v192, v160, v161\n"
    "ds_read_b128 v[144:147], v240 offset:10240\n"
    "v_exp_f32 v164, v164\n"
    "v_exp_f32 v165, v165\n"
    "ds_read_b128 v[148:151], v240 offset:11264\n"
    "v_mfma_f32_32x32x16_bf16 %1, v[152:155], v[200:203], %1\n"
    "v_cvt_pk_bf16_f32 v193, v162, v163\n"
    "v_exp_f32 v166, v166\n"
    "v_exp_f32 v167, v167\n"
    "v_cvt_pk_bf16_f32 v194, v164, v165\n"
    "v_mfma_f32_16x16x32_bf16 %3, %8, v[200:203], %3\n"
    "v_exp_f32 v168, v168\n"
    "v_exp_f32 v169, v169\n"
    "v_mfma_f32_32x32x16_bf16 %1, v[156:159], v[204:207], %1\n"
    "v_cvt_pk_bf16_f32 v195, v166, v167\n"
    "v_exp_f32 v170, v170\n"
    "v_exp_f32 v171, v171\n"
    "v_cvt_pk_bf16_f32 v196, v168, v169\n"
    "v_mfma_f32_16x16x32_bf16 %3, %8, v[204:207], %3\n"
    "v_exp_f32 v172, v172\n"
    "v_exp_f32 v173, v173\n"
    "v_cvt_pk_bf16_f32 v197, v170, v171\n"
    "v_exp_f32 v174, v174\n"
    "v_exp_f32 v175, v175\n"
    "v_cvt_pk_bf16_f32 v198, v172, v173\n"
    "v_cvt_pk_bf16_f32 v199, v174, v175\n"
    "s_waitcnt lgkmcnt(0)\n"
    "v_mfma_f32_32x32x16_bf16 v[160:175], v[136:139], %4, v[208:223]\n"
    "v_exp_f32 v176, v176\n"
    "v_exp_f32 v177, v177\n"
    "v_exp_f32 v178, v178\n"
    "v_mfma_f32_32x32x16_bf16 v[160:175], v[140:143], %5, v[160:175]\n"
    "v_exp_f32 v179, v179\n"
    "v_cvt_pk_bf16_f32 v200, v176, v177\n"
    "v_exp_f32 v180, v180\n"
    "v_exp_f32 v181, v181\n"
    "v_mfma_f32_32x32x16_bf16 %0, v[144:147], v[192:195], %0\n"
    "v_cvt_pk_bf16_f32 v201, v178, v179\n"
    "v_exp_f32 v182, v182\n"
    "v_exp_f32 v183, v183\n"
    "v_cvt_pk_bf16_f32 v202, v180, v181\n"
    "v_mfma_f32_16x16x32_bf16 %2, %8, v[192:195], %2\n"
    "v_exp_f32 v184, v184\n"
    "v_exp_f32 v185, v185\n"
    "v_mfma_f32_32x32x16_bf16 %0, v[148:151], v[196:199], %0\n"
    "v_cvt_pk_bf16_f32 v203, v182, v183\n"
    "v_exp_f32 v186, v186\n"
    "v_exp_f32 v187, v187\n"
    "v_cvt_pk_bf16_f32 v204, v184, v185\n"
    "v_mfma_f32_16x16x32_bf16 %2, %8, v[196:199], %2\n"
    "v_exp_f32 v188, v188\n"
    "v_exp_f32 v189, v189\n"
    "v_cvt_pk_bf16_f32 v205, v186, v187\n"
    "v_exp_f32 v190, v190\n"
    "v_exp_f32 v191, v191\n"
    "v_cvt_pk_bf16_f32 v206, v188, v189\n"
    "v_cvt_pk_bf16_f32 v207, v190, v191\n"
    "s_waitcnt vmcnt(4)\n"
    "s_barrier\n"
    "v_mfma_f32_32x32x16_bf16 v[176:191], v[136:139], %6, v[224:239]\n"
    "ds_read_b128 v[128:131], v240 offset:32768\n"
    "v_exp_f32 v160, v160\n"
    "v_exp_f32 v161, v161\n"
    "v_exp_f32 v162, v162\n"
    "ds_read_b128 v[132:135], v240 offset:33792\n"
    "v_mfma_f32_32x32x16_bf16 v[176:191], v[140:143], %7, v[176:191]\n"
    "v_exp_f32 v163, v163\n"
    "v_cvt_pk_bf16_f32 v192, v160, v161\n"
    "ds_read_b128 v[152:155], v240 offset:14336\n"
    "v_exp_f32 v164, v164\n"
    "v_exp_f32 v165, v165\n"
    "ds_read_b128 v[156:159], v240 offset:15360\n"
    "v_mfma_f32_32x32x16_bf16 %1, v[144:147], v[200:203], %1\n"
    "v_cvt_pk_bf16_f32 v193, v162, v163\n"
    "v_exp_f32 v166, v166\n"
    "v_exp_f32 v167, v167\n"
    "v_cvt_pk_bf16_f32 v194, v164, v165\n"
    "s_add_u32 m0, %12, 32768\n"
    "v_mfma_f32_16x16x32_bf16 %3, %8, v[200:203], %3\n"
    "v_exp_f32 v168, v168\n"
    "v_exp_f32 v169, v169\n"
    "buffer_load_dwordx4 %10, %11, s40 offen offset:0 sc1 lds\n"
    "v_mfma_f32_32x32x16_bf16 %1, v[148:151], v[204:207], %1\n"
    "v_cvt_pk_bf16_f32 v195, v166, v167\n"
    "v_exp_f32 v170, v170\n"
    "v_exp_f32 v171, v171\n"
    "v_cvt_pk_bf16_f32 v196, v168, v169\n"
    "buffer_load_dwordx4 %10, %11, s40 offen offset:1024 sc1 lds\n"
    "v_mfma_f32_16x16x32_bf16 %3, %8, v[204:207], %3\n"
    "v_exp_f32 v172, v172\n"
    "v_exp_f32 v173, v173\n"
    "v_cvt_pk_bf16_f32 v197, v170, v171\n"
    "v_exp_f32 v174, v174\n"
    "v_exp_f32 v175, v175\n"
    "v_cvt_pk_bf16_f32 v198, v172, v173\n"
    "v_cvt_pk_bf16_f32 v199, v174, v175\n"
    "s_waitcnt lgkmcnt(0)\n"
    "v_mfma_f32_32x32x16_bf16 v[160:175], v[128:131], %4, v[208:223]\n"
    "buffer_load_dwordx4 %10, %11, s40 offen offset:2048 sc1 lds\n"
    "v_exp_f32 v176, v176\n"
    "v_exp_f32 v177, v177\n"
    "v_exp_f32 v178, v178\n"
    "buffer_load_dwordx4 %10, %11, s40 offen offset:3072 sc1 lds\n"
    "v_mfma_f32_32x32x16_bf16 v[160:175], v[132:135], %5, v[160:175]\n"
    "v_exp_f32 v179, v179\n"
    "v_cvt_pk_bf16_f32 v200, v176, v177\n"
    "s_add_u32 s40, s40, 16384\n"
    "v_exp_f32 v180, v180\n"
    "v_exp_f32 v181, v181\n"
    "s_and_b32 s40, s40, 0x3ffff\n"
    "v_mfma_f32_32x32x16_bf16 %0, v[152:155], v[192:195], %0\n"
    "v_cvt_pk_bf16_f32 v201, v178, v179\n"
    "v_exp_f32 v182, v182\n"
    "v_exp_f32 v183, v183\n"
    "v_cvt_pk_bf16_f32 v202, v180, v181\n"
    "v_mfma_f32_16x16x32_bf16 %2, %8, v[192:195], %2\n"
    "v_exp_f32 v184, v184\n"
    "v_exp_f32 v185, v185\n"
    "v_mfma_f32_32x32x16_bf16 %0, v[156:159], v[196:199], %0\n"
    "v_cvt_pk_bf16_f32 v203, v182, v183\n"
    "v_exp_f32 v186, v186\n"
    "v_exp_f32 v187, v187\n"
    "v_cvt_pk_bf16_f32 v204, v184, v185\n"
    "v_mfma_f32_16x16x32_bf16 %2, %8, v[196:199], %2\n"
    "v_exp_f32 v188, v188\n"
    "v_exp_f32 v189, v189\n"
    "v_cvt_pk_bf16_f32 v205, v186, v187\n"
    "v_exp_f32 v190, v190\n"
    "v_exp_f32 v191, v191\n"
    "v_cvt_pk_bf16_f32 v206, v188, v189\n"
    "v_cvt_pk_bf16_f32 v207, v190, v191\n"
    "v_mfma_f32_32x32x16_bf16 v[176:191], v[128:131], %6, v[224:239]\n"
    "ds_read_b128 v[136:139], v240 offset:36864\n"
    "v_exp_f32 v160, v160\n"
    "v_exp_f32 v161, v161\n"
    "v_exp_f32 v162, v162\n"
    "ds_read_b128 v[140:143], v240 offset:37888\n"
    "v_mfma_f32_32x32x16_bf16 v[176:191], v[132:135], %7, v[176:191]\n"
    "v_exp_f32 v163, v163\n"
    "v_cvt_pk_bf16_f32 v192, v160, v161\n"
    "ds_read_b128 v[144:147], v240 offset:34816\n"
    "v_exp_f32 v164, v164\n"
    "v_exp_f32 v165, v165\n"
    "ds_read_b128 v[148:151], v240 offset:35840\n"
    "v_mfma_f32_32x32x16_bf16 %1, v[152:155], v[200:203], %1\n"
    "v_cvt_pk_bf16_f32 v193, v162, v163\n"
    "v_exp_f32 v166, v166\n"
    "v_exp_f32 v167, v167\n"
    "v_cvt_pk_bf16_f32 v194, v164, v165\n"
    "v_mfma_f32_16x16x32_bf16 %3, %8, v[200:203], %3\n"
    "v_exp_f32 v168, v168\n"
    "v_exp_f32 v169, v169\n"
    "v_mfma_f32_32x32x16_bf16 %1, v[156:159], v[204:207], %1\n"
    "v_cvt_pk_bf16_f32 v195, v166, v167\n"
    "v_exp_f32 v170, v170\n"
    "v_exp_f32 v171, v171\n"
    "v_cvt_pk_bf16_f32 v196, v168, v169\n"
    "v_mfma_f32_16x16x32_bf16 %3, %8, v[204:207], %3\n"
    "v_exp_f32 v172, v172\n"
    "v_exp_f32 v173, v173\n"
    "v_cvt_pk_bf16_f32 v197, v170, v171\n"
    "v_exp_f32 v174, v174\n"
    "v_exp_f32 v175, v175\n"
    "v_cvt_pk_bf16_f32 v198, v172, v173\n"
    "v_cvt_pk_bf16_f32 v199, v174, v175\n"
    "s_waitcnt lgkmcnt(0)\n"
    "v_mfma_f32_32x32x16_bf16 v[160:175], v[136:139], %4, v[208:223]\n"
    "v_exp_f32 v176, v176\n"
    "v_exp_f32 v177, v177\n"
    "v_exp_f32 v178, v178\n"
    "v_mfma_f32_32x32x16_bf16 v[160:175], v[140:143], %5, v[160:175]\n"
    "v_exp_f32 v179, v179\n"
    "v_cvt_pk_bf16_f32 v200, v176, v177\n"
    "v_exp_f32 v180, v180\n"
    "v_exp_f32 v181, v181\n"
    "v_mfma_f32_32x32x16_bf16 %0, v[144:147], v[192:195], %0\n"
    "v_cvt_pk_bf16_f32 v201, v178, v179\n"
    "v_exp_f32 v182, v182\n"
    "v_exp_f32 v183, v183\n"
    "v_cvt_pk_bf16_f32 v202, v180, v181\n"
    "v_mfma_f32_16x16x32_bf16 %2, %8, v[192:195], %2\n"
    "v_exp_f32 v184, v184\n"
    "v_exp_f32 v185, v185\n"
    "v_mfma_f32_32x32x16_bf16 %0, v[148:151], v[196:199], %0\n"
    "v_cvt_pk_bf16_f32 v203, v182, v183\n"
    "v_exp_f32 v186, v186\n"
    "v_exp_f32 v187, v187\n"
    "v_cvt_pk_bf16_f32 v204, v184, v185\n"
    "v_mfma_f32_16x16x32_bf16 %2, %8, v[196:199], %2\n"
    "v_exp_f32 v188, v188\n"
    "v_exp_f32 v189, v189\n"
    "v_cvt_pk_bf16_f32 v205, v186, v187\n"
    "v_exp_f32 v190, v190\n"
    "v_exp_f32 v191, v191\n"
    "v_cvt_pk_bf16_f32 v206, v188, v189\n"
    "v_cvt_pk_bf16_f32 v207, v190, v191\n"
    "v_mfma_f32_32x32x16_bf16 v[176:191], v[136:139], %6, v[224:239]\n"
    "ds_read_b128 v[128:131], v240 offset:40960\n"
    "v_exp_f32 v160, v160\n"
    "v_exp_f32 v161, v161\n"
    "v_exp_f32 v162, v162\n"
    "ds_read_b128 v[132:135], v240 offset:41984\n"
    "v_mfma_f32_32x32x16_bf16 v[176:191], v[140:143], %7, v[176:191]\n"
    "v_exp_f32 v163, v163\n"
    "v_cvt_pk_bf16_f32 v192, v160, v161\n"
    "ds_read_b128 v[152:155], v240 offset:38912\n"
    "v_exp_f32 v164, v164\n"
    "v_exp_f32 v165, v165\n"
    "ds_read_b128 v[156:159], v240 offset:39936\n"
    "v_mfma_f32_32x32x16_bf16 %1, v[144:147], v[200:203], %1\n"
    "v_cvt_pk_bf16_f32 v193, v162, v163\n"
    "v_exp_f32 v166, v166\n"
    "v_exp_f32 v167, v167\n"
    "v_cvt_pk_bf16_f32 v194, v164, v165\n"
    "v_mfma_f32_16x16x32_bf16 %3, %8, v[200:203], %3\n"
    "v_exp_f32 v168, v168\n"
    "v_exp_f32 v169, v169\n"
    "v_mfma_f32_32x32x16_bf16 %1, v[148:151], v[204:207], %1\n"
    "v_cvt_pk_bf16_f32 v195, v166, v167\n"
    "v_exp_f32 v170, v170\n"
    "v_exp_f32 v171, v171\n"
    "v_cvt_pk_bf16_f32 v196, v168, v169\n"
    "v_mfma_f32_16x16x32_bf16 %3, %8, v[204:207], %3\n"
    "v_exp_f32 v172, v172\n"
    "v_exp_f32 v173, v173\n"
    "v_cvt_pk_bf16_f32 v197, v170, v171\n"
    "v_exp_f32 v174, v174\n"
    "v_exp_f32 v175, v175\n"
    "v_cvt_pk_bf16_f32 v198, v172, v173\n"
    "v_cvt_pk_bf16_f32 v199, v174, v175\n"
    "s_waitcnt lgkmcnt(0)\n"
    "v_mfma_f32_32x32x16_bf16 v[160:175], v[128:131], %4, v[208:223]\n"
    "v_exp_f32 v176, v176\n"
    "v_exp_f32 v177, v177\n"
    "v_exp_f32 v178, v178\n"
    "v_mfma_f32_32x32x16_bf16 v[160:175], v[132:135], %5, v[160:175]\n"
    "v_exp_f32 v179, v179\n"
    "v_cvt_pk_bf16_f32 v200, v176, v177\n"
    "v_exp_f32 v180, v180\n"
    "v_exp_f32 v181, v181\n"
    "v_mfma_f32_32x32x16_bf16 %0, v[152:155], v[192:195], %0\n"
    "v_cvt_pk_bf16_f32 v201, v178, v179\n"
    "v_exp_f32 v182, v182\n"
    "v_exp_f32 v183, v183\n"
    "v_cvt_pk_bf16_f32 v202, v180, v181\n"
    "v_mfma_f32_16x16x32_bf16 %2, %8, v[192:195], %2\n"
    "v_exp_f32 v184, v184\n"
    "v_exp_f32 v185, v185\n"
    "v_mfma_f32_32x32x16_bf16 %0, v[156:159], v[196:199], %0\n"
    "v_cvt_pk_bf16_f32 v203, v182, v183\n"
    "v_exp_f32 v186, v186\n"
    "v_exp_f32 v187, v187\n"
    "v_cvt_pk_bf16_f32 v204, v184, v185\n"
    "v_mfma_f32_16x16x32_bf16 %2, %8, v[196:199], %2\n"
    "v_exp_f32 v188, v188\n"
    "v_exp_f32 v189, v189\n"
    "v_cvt_pk_bf16_f32 v205, v186, v187\n"
    "v_exp_f32 v190, v190\n"
    "v_exp_f32 v191, v191\n"
    "v_cvt_pk_bf16_f32 v206, v188, v189\n"
    "v_cvt_pk_bf16_f32 v207, v190, v191\n"
    "v_mfma_f32_32x32x16_bf16 v[176:191], v[128:131], %6, v[224:239]\n"
    "ds_read_b128 v[136:139], v240 offset:45056\n"
    "v_exp_f32 v160, v160\n"
    "v_exp_f32 v161, v161\n"
    "v_exp_f32 v162, v162\n"
    "ds_read_b128 v[140:143], v240 offset:46080\n"
    "v_mfma_f32_32x32x16_bf16 v[176:191], v[132:135], %7, v[176:191]\n"
    "v_exp_f32 v163, v163\n"
    "v_cvt_pk_bf16_f32 v192, v160, v161\n"
    "ds_read_b128 v[144:147], v240 offset:43008\n"
    "v_exp_f32 v164, v164\n"
    "v_exp_f32 v165, v165\n"
    "ds_read_b128 v[148:151], v240 offset:44032\n"
    "v_mfma_f32_32x32x16_bf16 %1, v[152:155], v[200:203], %1\n"
    "v_cvt_pk_bf16_f32 v193, v162, v163\n"
    "v_exp_f32 v166, v166\n"
    "v_exp_f32 v167, v167\n"
    "v_cvt_pk_bf16_f32 v194, v164, v165\n"
    "v_mfma_f32_16x16x32_bf16 %3, %8, v[200:203], %3\n"
    "v_exp_f32 v168, v168\n"
    "v_exp_f32 v169, v169\n"
    "v_mfma_f32_32x32x16_bf16 %1, v[156:159], v[204:207], %1\n"
    "v_cvt_pk_bf16_f32 v195, v166, v167\n"
    "v_exp_f32 v170, v170\n"
    "v_exp_f32 v171, v171\n"
    "v_cvt_pk_bf16_f32 v196, v168, v169\n"
    "v_mfma_f32_16x16x32_bf16 %3, %8, v[204:207], %3\n"
    "v_exp_f32 v172, v172\n"
    "v_exp_f32 v173, v173\n"
    "v_cvt_pk_bf16_f32 v197, v170, v171\n"
    "v_exp_f32 v174, v174\n"
    "v_exp_f32 v175, v175\n"
    "v_cvt_pk_bf16_f32 v198, v172, v173\n"
    "v_cvt_pk_bf16_f32 v199, v174, v175\n"
    "s_waitcnt lgkmcnt(0)\n"
    "v_mfma_f32_32x32x16_bf16 v[160:175], v[136:139], %4, v[208:223]\n"
    "v_exp_f32 v176, v176\n"
    "v_exp_f32 v177, v177\n"
    "v_exp_f32 v178, v178\n"
    "v_mfma_f32_32x32x16_bf16 v[160:175], v[140:143], %5, v[160:175]\n"
    "v_exp_f32 v179, v179\n"
    "v_cvt_pk_bf16_f32 v200, v176, v177\n"
    "v_exp_f32 v180, v180\n"
    "v_exp_f32 v181, v181\n"
    "v_mfma_f32_32x32x16_bf16 %0, v[144:147], v[192:195], %0\n"
    "v_cvt_pk_bf16_f32 v201, v178, v179\n"
    "v_exp_f32 v182, v182\n"
    "v_exp_f32 v183, v183\n"
    "v_cvt_pk_bf16_f32 v202, v180, v181\n"
    "v_mfma_f32_16x16x32_bf16 %2, %8, v[192:195], %2\n"
    "v_exp_f32 v184, v184\n"
    "v_exp_f32 v185, v185\n"
    "v_mfma_f32_32x32x16_bf16 %0, v[148:151], v[196:199], %0\n"
    "v_cvt_pk_bf16_f32 v203, v182, v183\n"
    "v_exp_f32 v186, v186\n"
    "v_exp_f32 v187, v187\n"
    "v_cvt_pk_bf16_f32 v204, v184, v185\n"
    "v_mfma_f32_16x16x32_bf16 %2, %8, v[196:199], %2\n"
    "v_exp_f32 v188, v188\n"
    "v_exp_f32 v189, v189\n"
    "v_cvt_pk_bf16_f32 v205, v186, v187\n"
    "v_exp_f32 v190, v190\n"
    "v_exp_f32 v191, v191\n"
    "v_cvt_pk_bf16_f32 v206, v188, v189\n"
    "v_cvt_pk_bf16_f32 v207, v190, v191\n"
    "s_waitcnt vmcnt(4)\n"
    "s_barrier\n"
    "v_mfma_f32_32x32x16_bf16 v[176:191], v[136:139], %6, v[224:239]\n"
    "ds_read_b128 v[128:131], %9 offset:0\n"
    "v_exp_f32 v160, v160\n"
    "v_exp_f32 v161, v161\n"
    "v_exp_f32 v162, v162\n"
    "ds_read_b128 v[132:135], %9 offset:1024\n"
    "v_mfma_f32_32x32x16_bf16 v[176:191], v[140:143], %7, v[176:191]\n"
    "v_exp_f32 v163, v163\n"
    "v_cvt_pk_bf16_f32 v192, v160, v161\n"
    "ds_read_b128 v[152:155], v240 offset:47104\n"
    "v_exp_f32 v164, v164\n"
    "v_exp_f32 v165, v165\n"
    "ds_read_b128 v[156:159], v240 offset:48128\n"
    "v_mfma_f32_32x32x16_bf16 %1, v[144:147], v[200:203], %1\n"
    "v_cvt_pk_bf16_f32 v193, v162, v163\n"
    "v_exp_f32 v166, v166\n"
    "v_exp_f32 v167, v167\n"
    "v_cvt_pk_bf16_f32 v194, v164, v165\n"
    "s_add_u32 m0, %12, 65536\n"
    "v_mfma_f32_16x16x32_bf16 %3, %8, v[200:203], %3\n"
    "v_exp_f32 v168, v168\n"
    "v_exp_f32 v169, v169\n"
    "buffer_load_dwordx4 %10, %11, s40 offen offset:0 sc1 lds\n"
    "v_mfma_f32_32x32x16_bf16 %1, v[148:151], v[204:207], %1\n"
    "v_cvt_pk_bf16_f32 v195, v166, v167\n"
    "v_exp_f32 v170, v170\n"
    "v_exp_f32 v171, v171\n"
    "v_cvt_pk_bf16_f32 v196, v168, v169\n"
    "buffer_load_dwordx4 %10, %11, s40 offen offset:1024 sc1 lds\n"
    "v_mfma_f32_16x16x32_bf16 %3, %8, v[204:207], %3\n"
    "v_exp_f32 v172, v172\n"
    "v_exp_f32 v173, v173\n"
    "v_cvt_pk_bf16_f32 v197, v170, v171\n"
    "v_exp_f32 v174, v174\n"
    "v_exp_f32 v175, v175\n"
    "v_cvt_pk_bf16_f32 v198, v172, v173\n"
    "v_cvt_pk_bf16_f32 v199, v174, v175\n"
    "s_waitcnt lgkmcnt(0)\n"
    "v_mfma_f32_32x32x16_bf16 v[160:175], v[128:131], %4, v[208:223]\n"
    "buffer_load_dwordx4 %10, %11, s40 offen offset:2048 sc1 lds\n"
    "v_exp_f32 v176, v176\n"
    "v_exp_f32 v177, v177\n"
    "v_exp_f32 v178, v178\n"
    "buffer_load_dwordx4 %10, %11, s40 offen offset:3072 sc1 lds\n"
    "v_mfma_f32_32x32x16_bf16 v[160:175], v[132:135], %5, v[160:175]\n"
    "v_exp_f32 v179, v179\n"
    "v_cvt_pk_bf16_f32 v200, v176, v177\n"
    "s_add_u32 s40, s40, 16384\n"
    "v_exp_f32 v180, v180\n"
    "v_exp_f32 v181, v181\n"
    "s_and_b32 s40, s40, 0x3ffff\n"
    "v_mfma_f32_32x32x16_bf16 %0, v[152:155], v[192:195], %0\n"
    "v_cvt_pk_bf16_f32 v201, v178, v179\n"
    "v_exp_f32 v182, v182\n"
    "v_exp_f32 v183, v183\n"
    "v_cvt_pk_bf16_f32 v202, v180, v181\n"
    "v_mfma_f32_16x16x32_bf16 %2, %8, v[192:195], %2\n"
    "v_exp_f32 v184, v184\n"
    "v_exp_f32 v185, v185\n"
    "v_mfma_f32_32x32x16_bf16 %0, v[156:159], v[196:199], %0\n"
    "v_cvt_pk_bf16_f32 v203, v182, v183\n"
    "v_exp_f32 v186, v186\n"
    "v_exp_f32 v187, v187\n"
    "v_cvt_pk_bf16_f32 v204, v184, v185\n"
    "v_mfma_f32_16x16x32_bf16 %2, %8, v[196:199], %2\n"
    "v_exp_f32 v188, v188\n"
    "v_exp_f32 v189, v189\n"
    "v_cvt_pk_bf16_f32 v205, v186, v187\n"
    "v_exp_f32 v190, v190\n"
    "v_exp_f32 v191, v191\n"
    "v_cvt_pk_bf16_f32 v206, v188, v189\n"
    "v_cvt_pk_bf16_f32 v207, v190, v191\n"
    "v_mfma_f32_32x32x16_bf16 v[176:191], v[128:131], %6, v[224:239]\n"
    "ds_read_b128 v[136:139], %9 offset:4096\n"
    "v_exp_f32 v160, v160\n"
    "v_exp_f32 v161, v161\n"
    "v_exp_f32 v162, v162\n"
    "ds_read_b128 v[140:143], %9 offset:5120\n"
    "v_mfma_f32_32x32x16_bf16 v[176:191], v[132:135], %7, v[176:191]\n"
    "v_exp_f32 v163, v163\n"
    "v_cvt_pk_bf16_f32 v192, v160, v161\n"
    "ds_read_b128 v[144:147], %9 offset:2048\n"
    "v_exp_f32 v164, v164\n"
    "v_exp_f32 v165, v165\n"
    "ds_read_b128 v[148:151], %9 offset:3072\n"
    "v_mfma_f32_32x32x16_bf16 %1, v[152:155], v[200:203], %1\n"
    "v_cvt_pk_bf16_f32 v193, v162, v163\n"
    "v_exp_f32 v166, v166\n"
    "v_exp_f32 v167, v167\n"
    "v_cvt_pk_bf16_f32 v194, v164, v165\n"
    "v_mfma_f32_16x16x32_bf16 %3, %8, v[200:203], %3\n"
    "v_exp_f32 v168, v168\n"
    "v_exp_f32 v169, v169\n"
    "v_mfma_f32_32x32x16_bf16 %1, v[156:159], v[204:207], %1\n"
    "v_cvt_pk_bf16_f32 v195, v166, v167\n"
    "v_exp_f32 v170, v170\n"
    "v_exp_f32 v171, v171\n"
    "v_cvt_pk_bf16_f32 v196, v168, v169\n"
    "v_mfma_f32_16x16x32_bf16 %3, %8, v[204:207], %3\n"
    "v_exp_f32 v172, v172\n"
    "v_exp_f32 v173, v173\n"
    "v_cvt_pk_bf16_f32 v197, v170, v171\n"
    "v_exp_f32 v174, v174\n"
    "v_exp_f32 v175, v175\n"
    "v_cvt_pk_bf16_f32 v198, v172, v173\n"
    "v_cvt_pk_bf16_f32 v199, v174, v175\n"
    "s_waitcnt lgkmcnt(0)\n"
    "v_mfma_f32_32x32x16_bf16 v[160:175], v[136:139], %4, v[208:223]\n"
    "v_exp_f32 v176, v176\n"
    "v_exp_f32 v177, v177\n"
    "v_exp_f32 v178, v178\n"
    "v_mfma_f32_32x32x16_bf16 v[160:175], v[140:143], %5, v[160:175]\n"
    "v_exp_f32 v179, v179\n"
    "v_cvt_pk_bf16_f32 v200, v176, v177\n"
    "v_exp_f32 v180, v180\n"
    "v_exp_f32 v181, v181\n"
    "v_mfma_f32_32x32x16_bf16 %0, v[144:147], v[192:195], %0\n"
    "v_cvt_pk_bf16_f32 v201, v178, v179\n"
    "v_exp_f32 v182, v182\n"
    "v_exp_f32 v183, v183\n"
    "v_cvt_pk_bf16_f32 v202, v180, v181\n"
    "v_mfma_f32_16x16x32_bf16 %2, %8, v[192:195], %2\n"
    "v_exp_f32 v184, v184\n"
    "v_exp_f32 v185, v185\n"
    "v_mfma_f32_32x32x16_bf16 %0, v[148:151], v[196:199], %0\n"
    "v_cvt_pk_bf16_f32 v203, v182, v183\n"
    "v_exp_f32 v186, v186\n"
    "v_exp_f32 v187, v187\n"
    "v_cvt_pk_bf16_f32 v204, v184, v185\n"
    "v_mfma_f32_16x16x32_bf16 %2, %8, v[196:199], %2\n"
    "v_exp_f32 v188, v188\n"
    "v_exp_f32 v189, v189\n"
    "v_cvt_pk_bf16_f32 v205, v186, v187\n"
    "v_exp_f32 v190, v190\n"
    "v_exp_f32 v191, v191\n"
    "v_cvt_pk_bf16_f32 v206, v188, v189\n"
    "v_cvt_pk_bf16_f32 v207, v190, v191\n"
    "v_mfma_f32_32x32x16_bf16 v[176:191], v[136:139], %6, v[224:239]\n"
    "ds_read_b128 v[128:131], %9 offset:8192\n"
    "v_exp_f32 v160, v160\n"
    "v_exp_f32 v161, v161\n"
    "v_exp_f32 v162, v162\n"
    "ds_read_b128 v[132:135], %9 offset:9216\n"
    "v_mfma_f32_32x32x16_bf16 v[176:191], v[140:143], %7, v[176:191]\n"
    "v_exp_f32 v163, v163\n"
    "v_cvt_pk_bf16_f32 v192, v160, v161\n"
    "ds_read_b128 v[152:155], %9 offset:6144\n"
    "v_exp_f32 v164, v164\n"
    "v_exp_f32 v165, v165\n"
    "ds_read_b128 v[156:159], %9 offset:7168\n"
    "v_mfma_f32_32x32x16_bf16 %1, v[144:147], v[200:203], %1\n"
    "v_cvt_pk_bf16_f32 v193, v162, v163\n"
    "v_exp_f32 v166, v166\n"
    "v_exp_f32 v167, v167\n"
    "v_cvt_pk_bf16_f32 v194, v164, v165\n"
    "v_mfma_f32_16x16x32_bf16 %3, %8, v[200:203], %3\n"
    "v_exp_f32 v168, v168\n"
    "v_exp_f32 v169, v169\n"
    "v_mfma_f32_32x32x16_bf16 %1, v[148:151], v[204:207], %1\n"
    "v_cvt_pk_bf16_f32 v195, v166, v167\n"
    "v_exp_f32 v170, v170\n"
    "v_exp_f32 v171, v171\n"
    "v_cvt_pk_bf16_f32 v196, v168, v169\n"
    "v_mfma_f32_16x16x32_bf16 %3, %8, v[204:207], %3\n"
    "v_exp_f32 v172, v172\n"
    "v_exp_f32 v173, v173\n"
    "v_cvt_pk_bf16_f32 v197, v170, v171\n"
    "v_exp_f32 v174, v174\n"
    "v_exp_f32 v175, v175\n"
    "v_cvt_pk_bf16_f32 v198, v172, v173\n"
    "v_cvt_pk_bf16_f32 v199, v174, v175\n"
    "s_waitcnt lgkmcnt(0)\n"
    "v_mfma_f32_32x32x16_bf16 v[160:175], v[128:131], %4, v[208:223]\n"
    "v_exp_f32 v176, v176\n"
    "v_exp_f32 v177, v177\n"
    "v_exp_f32 v178, v178\n"
    "v_mfma_f32_32x32x16_bf16 v[160:175], v[132:135], %5, v[160:175]\n"
    "v_exp_f32 v179, v179\n"
    "v_cvt_pk_bf16_f32 v200, v176, v177\n"
    "v_exp_f32 v180, v180\n"
    "v_exp_f32 v181, v181\n"
    "v_mfma_f32_32x32x16_bf16 %0, v[152:155], v[192:195], %0\n"
    "v_cvt_pk_bf16_f32 v201, v178, v179\n"
    "v_exp_f32 v182, v182\n"
    "v_exp_f32 v183, v183\n"
    "v_cvt_pk_bf16_f32 v202, v180, v181\n"
    "v_mfma_f32_16x16x32_bf16 %2, %8, v[192:195], %2\n"
    "v_exp_f32 v184, v184\n"
    "v_exp_f32 v185, v185\n"
    "v_mfma_f32_32x32x16_bf16 %0, v[156:159], v[196:199], %0\n"
    "v_cvt_pk_bf16_f32 v203, v182, v183\n"
    "v_exp_f32 v186, v186\n"
    "v_exp_f32 v187, v187\n"
    "v_cvt_pk_bf16_f32 v204, v184, v185\n"
    "v_mfma_f32_16x16x32_bf16 %2, %8, v[196:199], %2\n"
    "v_exp_f32 v188, v188\n"
    "v_exp_f32 v189, v189\n"
    "v_cvt_pk_bf16_f32 v205, v186, v187\n"
    "v_exp_f32 v190, v190\n"
    "v_exp_f32 v191, v191\n"
    "v_cvt_pk_bf16_f32 v206, v188, v189\n"
    "v_cvt_pk_bf16_f32 v207, v190, v191\n"
    "v_mfma_f32_32x32x16_bf16 v[176:191], v[128:131], %6, v[224:239]\n"
    "ds_read_b128 v[136:139], %9 offset:12288\n"
    "v_exp_f32 v160, v160\n"
    "v_exp_f32 v161, v161\n"
    "v_exp_f32 v162, v162\n"
    "ds_read_b128 v[140:143], %9 offset:13312\n"
    "v_mfma_f32_32x32x16_bf16 v[176:191], v[132:135], %7, v[176:191]\n"
    "v_exp_f32 v163, v163\n"
    "v_cvt_pk_bf16_f32 v192, v160, v161\n"
    "ds_read_b128 v[144:147], %9 offset:10240\n"
    "v_exp_f32 v164, v164\n"
    "v_exp_f32 v165, v165\n"
    "ds_read_b128 v[148:151], %9 offset:11264\n"
    "v_mfma_f32_32x32x16_bf16 %1, v[152:155], v[200:203], %1\n"
    "v_cvt_pk_bf16_f32 v193, v162, v163\n"
    "v_exp_f32 v166, v166\n"
    "v_exp_f32 v167, v167\n"
    "v_cvt_pk_bf16_f32 v194, v164, v165\n"
    "v_mfma_f32_16x16x32_bf16 %3, %8, v[200:203], %3\n"
    "v_exp_f32 v168, v168\n"
    "v_exp_f32 v169, v169\n"
    "v_mfma_f32_32x32x16_bf16 %1, v[156:159], v[204:207], %1\n"
    "v_cvt_pk_bf16_f32 v195, v166, v167\n"
    "v_exp_f32 v170, v170\n"
    "v_exp_f32 v171, v171\n"
    "v_cvt_pk_bf16_f32 v196, v168, v169\n"
    "v_mfma_f32_16x16x32_bf16 %3, %8, v[204:207], %3\n"
    "v_exp_f32 v172, v172\n"
    "v_exp_f32 v173, v173\n"
    "v_cvt_pk_bf16_f32 v197, v170, v171\n"
    "v_exp_f32 v174, v174\n"
    "v_exp_f32 v175, v175\n"
    "v_cvt_pk_bf16_f32 v198, v172, v173\n"
    "v_cvt_pk_bf16_f32 v199, v174, v175\n"
    "s_waitcnt lgkmcnt(0)\n"
    "v_mfma_f32_32x32x16_bf16 v[160:175], v[136:139], %4, v[208:223]\n"
    "v_exp_f32 v176, v176\n"
    "v_exp_f32 v177, v177\n"
    "v_exp_f32 v178, v178\n"
    "v_mfma_f32_32x32x16_bf16 v[160:175], v[140:143], %5, v[160:175]\n"
    "v_exp_f32 v179, v179\n"
    "v_cvt_pk_bf16_f32 v200, v176, v177\n"
    "v_exp_f32 v180, v180\n"
    "v_exp_f32 v181, v181\n"
    "v_mfma_f32_32x32x16_bf16 %0, v[144:147], v[192:195], %0\n"
    "v_cvt_pk_bf16_f32 v201, v178, v179\n"
    "v_exp_f32 v182, v182\n"
    "v_exp_f32 v183, v183\n"
    "v_cvt_pk_bf16_f32 v202, v180, v181\n"
    "v_mfma_f32_16x16x32_bf16 %2, %8, v[192:195], %2\n"
    "v_exp_f32 v184, v184\n"
    "v_exp_f32 v185, v185\n"
    "v_mfma_f32_32x32x16_bf16 %0, v[148:151], v[196:199], %0\n"
    "v_cvt_pk_bf16_f32 v203, v182, v183\n"
    "v_exp_f32 v186, v186\n"
    "v_exp_f32 v187, v187\n"
    "v_cvt_pk_bf16_f32 v204, v184, v185\n"
    "v_mfma_f32_16x16x32_bf16 %2, %8, v[196:199], %2\n"
    "v_exp_f32 v188, v188\n"
    "v_exp_f32 v189, v189\n"
    "v_cvt_pk_bf16_f32 v205, v186, v187\n"
    "v_exp_f32 v190, v190\n"
    "v_exp_f32 v191, v191\n"
    "v_cvt_pk_bf16_f32 v206, v188, v189\n"
    "v_cvt_pk_bf16_f32 v207, v190, v191\n"
    "s_waitcnt vmcnt(4)\n"
    "s_barrier\n"
    "v_mfma_f32_32x32x16_bf16 v[176:191], v[136:139], %6, v[224:239]\n"
    "ds_read_b128 v[128:131], %9 offset:32768\n"
    "v_exp_f32 v160, v160\n"
    "v_exp_f32 v161, v161\n"
    "v_exp_f32 v162, v162\n"
    "ds_read_b128 v[132:135], %9 offset:33792\n"
    "v_mfma_f32_32x32x16_bf16 v[176:191], v[140:143], %7, v[176:191]\n"
    "v_exp_f32 v163, v163\n"
    "v_cvt_pk_bf16_f32 v192, v160, v161\n"
    "ds_read_b128 v[152:155], %9 offset:14336\n"
    "v_exp_f32 v164, v164\n"
    "v_exp_f32 v165, v165\n"
    "ds_read_b128 v[156:159], %9 offset:15360\n"
    "v_mfma_f32_32x32x16_bf16 %1, v[144:147], v[200:203], %1\n"
    "v_cvt_pk_bf16_f32 v193, v162, v163\n"
    "v_exp_f32 v166, v166\n"
    "v_exp_f32 v167, v167\n"
    "v_cvt_pk_bf16_f32 v194, v164, v165\n"
    "s_add_u32 m0, %12, 98304\n"
    "v_mfma_f32_16x16x32_bf16 %3, %8, v[200:203], %3\n"
    "v_exp_f32 v168, v168\n"
    "v_exp_f32 v169, v169\n"
    "buffer_load_dwordx4 %10, %11, s40 offen offset:0 sc1 lds\n"
    "v_mfma_f32_32x32x16_bf16 %1, v[148:151], v[204:207], %1\n"
    "v_cvt_pk_bf16_f32 v195, v166, v167\n"
    "v_exp_f32 v170, v170\n"
    "v_exp_f32 v171, v171\n"
    "v_cvt_pk_bf16_f32 v196, v168, v169\n"
    "buffer_load_dwordx4 %10, %11, s40 offen offset:1024 sc1 lds\n"
    "v_mfma_f32_16x16x32_bf16 %3, %8, v[204:207], %3\n"
    "v_exp_f32 v172, v172\n"
    "v_exp_f32 v173, v173\n"
    "v_cvt_pk_bf16_f32 v197, v170, v171\n"
    "v_exp_f32 v174, v174\n"
    "v_exp_f32 v175, v175\n"
    "v_cvt_pk_bf16_f32 v198, v172, v173\n"
    "v_cvt_pk_bf16_f32 v199, v174, v175\n"
    "s_waitcnt lgkmcnt(0)\n"
    "v_mfma_f32_32x32x16_bf16 v[160:175], v[128:131], %4, v[208:223]\n"
    "buffer_load_dwordx4 %10, %11, s40 offen offset:2048 sc1 lds\n"
    "v_exp_f32 v176, v176\n"
    "v_exp_f32 v177, v177\n"
    "v_exp_f32 v178, v178\n"
    "buffer_load_dwordx4 %10, %11, s40 offen offset:3072 sc1 lds\n"
    "v_mfma_f32_32x32x16_bf16 v[160:175], v[132:135], %5, v[160:175]\n"
    "v_exp_f32 v179, v179\n"
    "v_cvt_pk_bf16_f32 v200, v176, v177\n"
    "s_add_u32 s40, s40, 16384\n"
    "v_exp_f32 v180, v180\n"
    "v_exp_f32 v181, v181\n"
    "s_and_b32 s40, s40, 0x3ffff\n"
    "v_mfma_f32_32x32x16_bf16 %0, v[152:155], v[192:195], %0\n"
    "v_cvt_pk_bf16_f32 v201, v178, v179\n"
    "v_exp_f32 v182, v182\n"
    "v_exp_f32 v183, v183\n"
    "v_cvt_pk_bf16_f32 v202, v180, v181\n"
    "v_mfma_f32_16x16x32_bf16 %2, %8, v[192:195], %2\n"
    "v_exp_f32 v184, v184\n"
    "v_exp_f32 v185, v185\n"
    "v_mfma_f32_32x32x16_bf16 %0, v[156:159], v[196:199], %0\n"
    "v_cvt_pk_bf16_f32 v203, v182, v183\n"
    "v_exp_f32 v186, v186\n"
    "v_exp_f32 v187, v187\n"
    "v_cvt_pk_bf16_f32 v204, v184, v185\n"
    "v_mfma_f32_16x16x32_bf16 %2, %8, v[196:199], %2\n"
    "v_exp_f32 v188, v188\n"
    "v_exp_f32 v189, v189\n"
    "v_cvt_pk_bf16_f32 v205, v186, v187\n"
    "v_exp_f32 v190, v190\n"
    "v_exp_f32 v191, v191\n"
    "v_cvt_pk_bf16_f32 v206, v188, v189\n"
    "v_cvt_pk_bf16_f32 v207, v190, v191\n"
    "v_mfma_f32_32x32x16_bf16 v[176:191], v[128:131], %6, v[224:239]\n"
    "ds_read_b128 v[136:139], %9 offset:36864\n"
    "v_exp_f32 v160, v160\n"
    "v_exp_f32 v161, v161\n"
    "v_exp_f32 v162, v162\n"
    "ds_read_b128 v[140:143], %9 offset:37888\n"
    "v_mfma_f32_32x32x16_bf16 v[176:191], v[132:135], %7, v[176:191]\n"
    "v_exp_f32 v163, v163\n"
    "v_cvt_pk_bf16_f32 v192, v160, v161\n"
    "ds_read_b128 v[144:147], %9 offset:34816\n"
    "v_exp_f32 v164, v164\n"
    "v_exp_f32 v165, v165\n"
    "ds_read_b128 v[148:151], %9 offset:35840\n"
    "v_mfma_f32_32x32x16_bf16 %1, v[152:155], v[200:203], %1\n"
    "v_cvt_pk_bf16_f32 v193, v162, v163\n"
    "v_exp_f32 v166, v166\n"
    "v_exp_f32 v167, v167\n"
    "v_cvt_pk_bf16_f32 v194, v164, v165\n"
    "v_mfma_f32_16x16x32_bf16 %3, %8, v[200:203], %3\n"
    "v_exp_f32 v168, v168\n"
    "v_exp_f32 v169, v169\n"
    "v_mfma_f32_32x32x16_bf16 %1, v[156:159], v[204:207], %1\n"
    "v_cvt_pk_bf16_f32 v195, v166, v167\n"
    "v_exp_f32 v170, v170\n"
    "v_exp_f32 v171, v171\n"
    "v_cvt_pk_bf16_f32 v196, v168, v169\n"
    "v_mfma_f32_16x16x32_bf16 %3, %8, v[204:207], %3\n"
    "v_exp_f32 v172, v172\n"
    "v_exp_f32 v173, v173\n"
    "v_cvt_pk_bf16_f32 v197, v170, v171\n"
    "v_exp_f32 v174, v174\n"
    "v_exp_f32 v175, v175\n"
    "v_cvt_pk_bf16_f32 v198, v172, v173\n"
    "v_cvt_pk_bf16_f32 v199, v174, v175\n"
    "s_waitcnt lgkmcnt(0)\n"
    "v_mfma_f32_32x32x16_bf16 v[160:175], v[136:139], %4, v[208:223]\n"
    "v_exp_f32 v176, v176\n"
    "v_exp_f32 v177, v177\n"
    "v_exp_f32 v178, v178\n"
    "v_mfma_f32_32x32x16_bf16 v[160:175], v[140:143], %5, v[160:175]\n"
    "v_exp_f32 v179, v179\n"
    "v_cvt_pk_bf16_f32 v200, v176, v177\n"
    "v_exp_f32 v180, v180\n"
    "v_exp_f32 v181, v181\n"
    "v_mfma_f32_32x32x16_bf16 %0, v[144:147], v[192:195], %0\n"
    "v_cvt_pk_bf16_f32 v201, v178, v179\n"
    "v_exp_f32 v182, v182\n"
    "v_exp_f32 v183, v183\n"
    "v_cvt_pk_bf16_f32 v202, v180, v181\n"
    "v_mfma_f32_16x16x32_bf16 %2, %8, v[192:195], %2\n"
    "v_exp_f32 v184, v184\n"
    "v_exp_f32 v185, v185\n"
    "v_mfma_f32_32x32x16_bf16 %0, v[148:151], v[196:199], %0\n"
    "v_cvt_pk_bf16_f32 v203, v182, v183\n"
    "v_exp_f32 v186, v186\n"
    "v_exp_f32 v187, v187\n"
    "v_cvt_pk_bf16_f32 v204, v184, v185\n"
    "v_mfma_f32_16x16x32_bf16 %2, %8, v[196:199], %2\n"
    "v_exp_f32 v188, v188\n"
    "v_exp_f32 v189, v189\n"
    "v_cvt_pk_bf16_f32 v205, v186, v187\n"
    "v_exp_f32 v190, v190\n"
    "v_exp_f32 v191, v191\n"
    "v_cvt_pk_bf16_f32 v206, v188, v189\n"
    "v_cvt_pk_bf16_f32 v207, v190, v191\n"
    "v_mfma_f32_32x32x16_bf16 v[176:191], v[136:139], %6, v[224:239]\n"
    "ds_read_b128 v[128:131], %9 offset:40960\n"
    "v_exp_f32 v160, v160\n"
    "v_exp_f32 v161, v161\n"
    "v_exp_f32 v162, v162\n"
    "ds_read_b128 v[132:135], %9 offset:41984\n"
    "v_mfma_f32_32x32x16_bf16 v[176:191], v[140:143], %7, v[176:191]\n"
    "v_exp_f32 v163, v163\n"
    "v_cvt_pk_bf16_f32 v192, v160, v161\n"
    "ds_read_b128 v[152:155], %9 offset:38912\n"
    "v_exp_f32 v164, v164\n"
    "v_exp_f32 v165, v165\n"
    "ds_read_b128 v[156:159], %9 offset:39936\n"
    "v_mfma_f32_32x32x16_bf16 %1, v[144:147], v[200:203], %1\n"
    "v_cvt_pk_bf16_f32 v193, v162, v163\n"
    "v_exp_f32 v166, v166\n"
    "v_exp_f32 v167, v167\n"
    "v_cvt_pk_bf16_f32 v194, v164, v165\n"
    "v_mfma_f32_16x16x32_bf16 %3, %8, v[200:203], %3\n"
    "v_exp_f32 v168, v168\n"
    "v_exp_f32 v169, v169\n"
    "v_mfma_f32_32x32x16_bf16 %1, v[148:151], v[204:207], %1\n"
    "v_cvt_pk_bf16_f32 v195, v166, v167\n"
    "v_exp_f32 v170, v170\n"
    "v_exp_f32 v171, v171\n"
    "v_cvt_pk_bf16_f32 v196, v168, v169\n"
    "v_mfma_f32_16x16x32_bf16 %3, %8, v[204:207], %3\n"
    "v_exp_f32 v172, v172\n"
    "v_exp_f32 v173, v173\n"
    "v_cvt_pk_bf16_f32 v197, v170, v171\n"
    "v_exp_f32 v174, v174\n"
    "v_exp_f32 v175, v175\n"
    "v_cvt_pk_bf16_f32 v198, v172, v173\n"
    "v_cvt_pk_bf16_f32 v199, v174, v175\n"
    "s_waitcnt lgkmcnt(0)\n"
    "v_mfma_f32_32x32x16_bf16 v[160:175], v[128:131], %4, v[208:223]\n"
    "v_exp_f32 v176, v176\n"
    "v_exp_f32 v177, v177\n"
    "v_exp_f32 v178, v178\n"
    "v_mfma_f32_32x32x16_bf16 v[160:175], v[132:135], %5, v[160:175]\n"
    "v_exp_f32 v179, v179\n"
    "v_cvt_pk_bf16_f32 v200, v176, v177\n"
    "v_exp_f32 v180, v180\n"
    "v_exp_f32 v181, v181\n"
    "v_mfma_f32_32x32x16_bf16 %0, v[152:155], v[192:195], %0\n"
    "v_cvt_pk_bf16_f32 v201, v178, v179\n"
    "v_exp_f32 v182, v182\n"
    "v_exp_f32 v183, v183\n"
    "v_cvt_pk_bf16_f32 v202, v180, v181\n"
    "v_mfma_f32_16x16x32_bf16 %2, %8, v[192:195], %2\n"
    "v_exp_f32 v184, v184\n"
    "v_exp_f32 v185, v185\n"
    "v_mfma_f32_32x32x16_bf16 %0, v[156:159], v[196:199], %0\n"
    "v_cvt_pk_bf16_f32 v203, v182, v183\n"
    "v_exp_f32 v186, v186\n"
    "v_exp_f32 v187, v187\n"
    "v_cvt_pk_bf16_f32 v204, v184, v185\n"
    "v_mfma_f32_16x16x32_bf16 %2, %8, v[196:199], %2\n"
    "v_exp_f32 v188, v188\n"
    "v_exp_f32 v189, v189\n"
    "v_cvt_pk_bf16_f32 v205, v186, v187\n"
    "v_exp_f32 v190, v190\n"
    "v_exp_f32 v191, v191\n"
    "v_cvt_pk_bf16_f32 v206, v188, v189\n"
    "v_cvt_pk_bf16_f32 v207, v190, v191\n"
    "v_mfma_f32_32x32x16_bf16 v[176:191], v[128:131], %6, v[224:239]\n"
    "ds_read_b128 v[136:139], %9 offset:45056\n"
    "v_exp_f32 v160, v160\n"
    "v_exp_f32 v161, v161\n"
    "v_exp_f32 v162, v162\n"
    "ds_read_b128 v[140:143], %9 offset:46080\n"
    "v_mfma_f32_32x32x16_bf16 v[176:191], v[132:135], %7, v[176:191]\n"
    "v_exp_f32 v163, v163\n"
    "v_cvt_pk_bf16_f32 v192, v160, v161\n"
    "ds_read_b128 v[144:147], %9 offset:43008\n"
    "v_exp_f32 v164, v164\n"
    "v_exp_f32 v165, v165\n"
    "ds_read_b128 v[148:151], %9 offset:44032\n"
    "v_mfma_f32_32x32x16_bf16 %1, v[152:155], v[200:203], %1\n"
    "v_cvt_pk_bf16_f32 v193, v162, v163\n"
    "v_exp_f32 v166, v166\n"
    "v_exp_f32 v167, v167\n"
    "v_cvt_pk_bf16_f32 v194, v164, v165\n"
    "v_mfma_f32_16x16x32_bf16 %3, %8, v[200:203], %3\n"
    "v_exp_f32 v168, v168\n"
    "v_exp_f32 v169, v169\n"
    "v_mfma_f32_32x32x16_bf16 %1, v[156:159], v[204:207], %1\n"
    "v_cvt_pk_bf16_f32 v195, v166, v167\n"
    "v_exp_f32 v170, v170\n"
    "v_exp_f32 v171, v171\n"
    "v_cvt_pk_bf16_f32 v196, v168, v169\n"
    "v_mfma_f32_16x16x32_bf16 %3, %8, v[204:207], %3\n"
    "v_exp_f32 v172, v172\n"
    "v_exp_f32 v173, v173\n"
    "v_cvt_pk_bf16_f32 v197, v170, v171\n"
    "v_exp_f32 v174, v174\n"
    "v_exp_f32 v175, v175\n"
    "v_cvt_pk_bf16_f32 v198, v172, v173\n"
    "v_cvt_pk_bf16_f32 v199, v174, v175\n"
    "s_waitcnt lgkmcnt(0)\n"
    "v_mfma_f32_32x32x16_bf16 v[160:175], v[136:139], %4, v[208:223]\n"
    "v_exp_f32 v176, v176\n"
    "v_exp_f32 v177, v177\n"
    "v_exp_f32 v178, v178\n"
    "v_mfma_f32_32x32x16_bf16 v[160:175], v[140:143], %5, v[160:175]\n"
    "v_exp_f32 v179, v179\n"
    "v_cvt_pk_bf16_f32 v200, v176, v177\n"
    "v_exp_f32 v180, v180\n"
    "v_exp_f32 v181, v181\n"
    "v_mfma_f32_32x32x16_bf16 %0, v[144:147], v[192:195], %0\n"
    "v_cvt_pk_bf16_f32 v201, v178, v179\n"
    "v_exp_f32 v182, v182\n"
    "v_exp_f32 v183, v183\n"
    "v_cvt_pk_bf16_f32 v202, v180, v181\n"
    "v_mfma_f32_16x16x32_bf16 %2, %8, v[192:195], %2\n"
    "v_exp_f32 v184, v184\n"
    "v_exp_f32 v185, v185\n"
    "v_mfma_f32_32x32x16_bf16 %0, v[148:151], v[196:199], %0\n"
    "v_cvt_pk_bf16_f32 v203, v182, v183\n"
    "v_exp_f32 v186, v186\n"
    "v_exp_f32 v187, v187\n"
    "v_cvt_pk_bf16_f32 v204, v184, v185\n"
    "v_mfma_f32_16x16x32_bf16 %2, %8, v[196:199], %2\n"
    "v_exp_f32 v188, v188\n"
    "v_exp_f32 v189, v189\n"
    "v_cvt_pk_bf16_f32 v205, v186, v187\n"
    "v_exp_f32 v190, v190\n"
    "v_exp_f32 v191, v191\n"
    "v_cvt_pk_bf16_f32 v206, v188, v189\n"
    "v_cvt_pk_bf16_f32 v207, v190, v191\n"
    "s_waitcnt vmcnt(4)\n"
    "s_barrier\n"
    "v_mfma_f32_32x32x16_bf16 v[176:191], v[136:139], %6, v[224:239]\n"
    "ds_read_b128 v[128:131], v240 offset:0\n"
    "v_exp_f32 v160, v160\n"
    "v_exp_f32 v161, v161\n"
    "v_exp_f32 v162, v162\n"
    "ds_read_b128 v[132:135], v240 offset:1024\n"
    "v_mfma_f32_32x32x16_bf16 v[176:191], v[140:143], %7, v[176:191]\n"
    "v_exp_f32 v163, v163\n"
    "v_cvt_pk_bf16_f32 v192, v160, v161\n"
    "ds_read_b128 v[152:155], %9 offset:47104\n"
    "v_exp_f32 v164, v164\n"
    "v_exp_f32 v165, v165\n"
    "ds_read_b128 v[156:159], %9 offset:48128\n"
    "v_mfma_f32_32x32x16_bf16 %1, v[144:147], v[200:203], %1\n"
    "v_cvt_pk_bf16_f32 v193, v162, v163\n"
    "v_exp_f32 v166, v166\n"
    "v_exp_f32 v167, v167\n"
    "v_cvt_pk_bf16_f32 v194, v164, v165\n"
    "s_add_u32 m0, %12, 0\n"
    "v_mfma_f32_16x16x32_bf16 %3, %8, v[200:203], %3\n"
    "v_exp_f32 v168, v168\n"
    "v_exp_f32 v169, v169\n"
    "buffer_load_dwordx4 %10, %11, s40 offen offset:0 sc1 lds\n"
    "v_mfma_f32_32x32x16_bf16 %1, v[148:151], v[204:207], %1\n"
    "v_cvt_pk_bf16_f32 v195, v166, v167\n"
    "v_exp_f32 v170, v170\n"
    "v_exp_f32 v171, v171\n"
    "v_cvt_pk_bf16_f32 v196, v168, v169\n"
    "buffer_load_dwordx4 %10, %11, s40 offen offset:1024 sc1 lds\n"
    "v_mfma_f32_16x16x32_bf16 %3, %8, v[204:207], %3\n"
    "v_exp_f32 v172, v172\n"
    "v_exp_f32 v173, v173\n"
    "v_cvt_pk_bf16_f32 v197, v170, v171\n"
    "v_exp_f32 v174, v174\n"
    "v_exp_f32 v175, v175\n"
    "v_cvt_pk_bf16_f32 v198, v172, v173\n"
    "v_cvt_pk_bf16_f32 v199, v174, v175\n"
    "s_waitcnt lgkmcnt(0)\n"
    "v_mfma_f32_32x32x16_bf16 v[160:175], v[128:131], %4, v[208:223]\n"
    "buffer_load_dwordx4 %10, %11, s40 offen offset:2048 sc1 lds\n"
    "v_exp_f32 v176, v176\n"
    "v_exp_f32 v177, v177\n"
    "v_exp_f32 v178, v178\n"
    "buffer_load_dwordx4 %10, %11, s40 offen offset:3072 sc1 lds\n"
    "v_mfma_f32_32x32x16_bf16 v[160:175], v[132:135], %5, v[160:175]\n"
    "v_exp_f32 v179, v179\n"
    "v_cvt_pk_bf16_f32 v200, v176, v177\n"
    "s_add_u32 s40, s40, 16384\n"
    "v_exp_f32 v180, v180\n"
    "v_exp_f32 v181, v181\n"
    "s_and_b32 s40, s40, 0x3ffff\n"
    "v_mfma_f32_32x32x16_bf16 %0, v[152:155], v[192:195], %0\n"
    "v_cvt_pk_bf16_f32 v201, v178, v179\n"
    "v_exp_f32 v182, v182\n"
    "v_exp_f32 v183, v183\n"
    "v_cvt_pk_bf16_f32 v202, v180, v181\n"
    "v_mfma_f32_16x16x32_bf16 %2, %8, v[192:195], %2\n"
    "v_exp_f32 v184, v184\n"
    "v_exp_f32 v185, v185\n"
    "v_mfma_f32_32x32x16_bf16 %0, v[156:159], v[196:199], %0\n"
    "v_cvt_pk_bf16_f32 v203, v182, v183\n"
    "v_exp_f32 v186, v186\n"
    "v_exp_f32 v187, v187\n"
    "v_cvt_pk_bf16_f32 v204, v184, v185\n"
    "v_mfma_f32_16x16x32_bf16 %2, %8, v[196:199], %2\n"
    "v_exp_f32 v188, v188\n"
    "v_exp_f32 v189, v189\n"
    "v_cvt_pk_bf16_f32 v205, v186, v187\n"
    "v_exp_f32 v190, v190\n"
    "v_exp_f32 v191, v191\n"
    "v_cvt_pk_bf16_f32 v206, v188, v189\n"
    "v_cvt_pk_bf16_f32 v207, v190, v191\n"
    "s_sub_u32 s42, s42, 1\n"
    "s_cbranch_scc0 Lattn_loop_%=\n"
    "v_mfma_f32_32x32x16_bf16 v[176:191], v[128:131], %6, v[224:239]\n"
    "ds_read_b128 v[136:139], v240 offset:4096\n"
    "v_exp_f32 v160, v160\n"
    "v_exp_f32 v161, v161\n"
    "v_exp_f32 v162, v162\n"
    "ds_read_b128 v[140:143], v240 offset:5120\n"
    "v_mfma_f32_32x32x16_bf16 v[176:191], v[132:135], %7, v[176:191]\n"
    "v_exp_f32 v163, v163\n"
    "v_cvt_pk_bf16_f32 v192, v160, v161\n"
    "ds_read_b128 v[144:147], v240 offset:2048\n"
    "v_exp_f32 v164, v164\n"
    "v_exp_f32 v165, v165\n"
    "ds_read_b128 v[148:151], v240 offset:3072\n"
    "v_mfma_f32_32x32x16_bf16 %1, v[152:155], v[200:203], %1\n"
    "v_cvt_pk_bf16_f32 v193, v162, v163\n"
    "v_exp_f32 v166, v166\n"
    "v_exp_f32 v167, v167\n"
    "v_cvt_pk_bf16_f32 v194, v164, v165\n"
    "v_mfma_f32_16x16x32_bf16 %3, %8, v[200:203], %3\n"
    "v_exp_f32 v168, v168\n"
    "v_exp_f32 v169, v169\n"
    "v_mfma_f32_32x32x16_bf16 %1, v[156:159], v[204:207], %1\n"
    "v_cvt_pk_bf16_f32 v195, v166, v167\n"
    "v_exp_f32 v170, v170\n"
    "v_exp_f32 v171, v171\n"
    "v_cvt_pk_bf16_f32 v196, v168, v169\n"
    "v_mfma_f32_16x16x32_bf16 %3, %8, v[204:207], %3\n"
    "v_exp_f32 v172, v172\n"
    "v_exp_f32 v173, v173\n"
    "v_cvt_pk_bf16_f32 v197, v170, v171\n"
    "v_exp_f32 v174, v174\n"
    "v_exp_f32 v175, v175\n"
    "v_cvt_pk_bf16_f32 v198, v172, v173\n"
    "v_cvt_pk_bf16_f32 v199, v174, v175\n"
    "s_waitcnt lgkmcnt(0)\n"
    "buffer_load_dword v242, %17, %18, 0 offen\n"
    "v_mfma_f32_32x32x16_bf16 v[160:175], v[136:139], %4, v[208:223]\n"
    "v_exp_f32 v176, v176\n"
    "v_exp_f32 v177, v177\n"
    "v_exp_f32 v178, v178\n"
    "v_mfma_f32_32x32x16_bf16 v[160:175], v[140:143], %5, v[160:175]\n"
    "v_exp_f32 v179, v179\n"
    "v_cvt_pk_bf16_f32 v200, v176, v177\n"
    "v_exp_f32 v180, v180\n"
    "v_exp_f32 v181, v181\n"
    "v_mfma_f32_32x32x16_bf16 %0, v[144:147], v[192:195], %0\n"
    "v_cvt_pk_bf16_f32 v201, v178, v179\n"
    "v_exp_f32 v182, v182\n"
    "v_exp_f32 v183, v183\n"
    "v_cvt_pk_bf16_f32 v202, v180, v181\n"
    "v_mfma_f32_16x16x32_bf16 %2, %8, v[192:195], %2\n"
    "v_exp_f32 v184, v184\n"
    "v_exp_f32 v185, v185\n"
    "v_mfma_f32_32x32x16_bf16 %0, v[148:151], v[196:199], %0\n"
    "v_cvt_pk_bf16_f32 v203, v182, v183\n"
    "v_exp_f32 v186, v186\n"
    "v_exp_f32 v187, v187\n"
    "v_cvt_pk_bf16_f32 v204, v184, v185\n"
    "v_mfma_f32_16x16x32_bf16 %2, %8, v[196:199], %2\n"
    "v_exp_f32 v188, v188\n"
    "v_exp_f32 v189, v189\n"
    "v_cvt_pk_bf16_f32 v205, v186, v187\n"
    "v_exp_f32 v190, v190\n"
    "v_exp_f32 v191, v191\n"
    "v_cvt_pk_bf16_f32 v206, v188, v189\n"
    "v_cvt_pk_bf16_f32 v207, v190, v191\n"
    "v_mfma_f32_32x32x16_bf16 v[176:191], v[136:139], %6, v[224:239]\n"
    "ds_read_b128 v[128:131], v240 offset:8192\n"
    "v_exp_f32 v160, v160\n"
    "v_exp_f32 v161, v161\n"
    "v_exp_f32 v162, v162\n"
    "ds_read_b128 v[132:135], v240 offset:9216\n"
    "v_mfma_f32_32x32x16_bf16 v[176:191], v[140:143], %7, v[176:191]\n"
    "v_exp_f32 v163, v163\n"
    "v_cvt_pk_bf16_f32 v192, v160, v161\n"
    "ds_read_b128 v[152:155], v240 offset:6144\n"
    "v_exp_f32 v164, v164\n"
    "v_exp_f32 v165, v165\n"
    "ds_read_b128 v[156:159], v240 offset:7168\n"
    "v_mfma_f32_32x32x16_bf16 %1, v[144:147], v[200:203], %1\n"
    "v_cvt_pk_bf16_f32 v193, v162, v163\n"
    "v_exp_f32 v166, v166\n"
    "v_exp_f32 v167, v167\n"
    "v_cvt_pk_bf16_f32 v194, v164, v165\n"
    "v_mfma_f32_16x16x32_bf16 %3, %8, v[200:203], %3\n"
    "v_exp_f32 v168, v168\n"
    "v_exp_f32 v169, v169\n"
    "v_mfma_f32_32x32x16_bf16 %1, v[148:151], v[204:207], %1\n"
    "v_cvt_pk_bf16_f32 v195, v166, v167\n"
    "v_exp_f32 v170, v170\n"
    "v_exp_f32 v171, v171\n"
    "v_cvt_pk_bf16_f32 v196, v168, v169\n"
    "v_mfma_f32_16x16x32_bf16 %3, %8, v[204:207], %3\n"
    "v_exp_f32 v172, v172\n"
    "v_exp_f32 v173, v173\n"
    "v_cvt_pk_bf16_f32 v197, v170, v171\n"
    "v_exp_f32 v174, v174\n"
    "v_exp_f32 v175, v175\n"
    "v_cvt_pk_bf16_f32 v198, v172, v173\n"
    "v_cvt_pk_bf16_f32 v199, v174, v175\n"
    "s_waitcnt lgkmcnt(0)\n"
    "v_mfma_f32_32x32x16_bf16 v[160:175], v[128:131], %4, v[208:223]\n"
    "v_exp_f32 v176, v176\n"
    "v_exp_f32 v177, v177\n"
    "v_exp_f32 v178, v178\n"
    "v_mfma_f32_32x32x16_bf16 v[160:175], v[132:135], %5, v[160:175]\n"
    "v_exp_f32 v179, v179\n"
    "v_cvt_pk_bf16_f32 v200, v176, v177\n"
    "v_exp_f32 v180, v180\n"
    "v_exp_f32 v181, v181\n"
    "v_mfma_f32_32x32x16_bf16 %0, v[152:155], v[192:195], %0\n"
    "v_cvt_pk_bf16_f32 v201, v178, v179\n"
    "v_exp_f32 v182, v182\n"
    "v_exp_f32 v183, v183\n"
    "v_cvt_pk_bf16_f32 v202, v180, v181\n"
    "v_mfma_f32_16x16x32_bf16 %2, %8, v[192:195], %2\n"
    "v_exp_f32 v184, v184\n"
    "v_exp_f32 v185, v185\n"
    "v_mfma_f32_32x32x16_bf16 %0, v[156:159], v[196:199], %0\n"
    "v_cvt_pk_bf16_f32 v203, v182, v183\n"
    "v_exp_f32 v186, v186\n"
    "v_exp_f32 v187, v187\n"
    "v_cvt_pk_bf16_f32 v204, v184, v185\n"
    "v_mfma_f32_16x16x32_bf16 %2, %8, v[196:199], %2\n"
    "v_exp_f32 v188, v188\n"
    "v_exp_f32 v189, v189\n"
    "v_cvt_pk_bf16_f32 v205, v186, v187\n"
    "v_exp_f32 v190, v190\n"
    "v_exp_f32 v191, v191\n"
    "v_cvt_pk_bf16_f32 v206, v188, v189\n"
    "v_cvt_pk_bf16_f32 v207, v190, v191\n"
    "v_mfma_f32_32x32x16_bf16 v[176:191], v[128:131], %6, v[224:239]\n"
    "ds_read_b128 v[136:139], v240 offset:12288\n"
    "v_exp_f32 v160, v160\n"
    "v_exp_f32 v161, v161\n"
    "v_exp_f32 v162, v162\n"
    "ds_read_b128 v[140:143], v240 offset:13312\n"
    "v_mfma_f32_32x32x16_bf16 v[176:191], v[132:135], %7, v[176:191]\n"
    "v_exp_f32 v163, v163\n"
    "v_cvt_pk_bf16_f32 v192, v160, v161\n"
    "ds_read_b128 v[144:147], v240 offset:10240\n"
    "v_exp_f32 v164, v164\n"
    "v_exp_f32 v165, v165\n"
    "ds_read_b128 v[148:151], v240 offset:11264\n"
    "v_mfma_f32_32x32x16_bf16 %1, v[152:155], v[200:203], %1\n"
    "v_cvt_pk_bf16_f32 v193, v162, v163\n"
    "v_exp_f32 v166, v166\n"
    "v_exp_f32 v167, v167\n"
    "v_cvt_pk_bf16_f32 v194, v164, v165\n"
    "v_mfma_f32_16x16x32_bf16 %3, %8, v[200:203], %3\n"
    "v_exp_f32 v168, v168\n"
    "v_exp_f32 v169, v169\n"
    "v_mfma_f32_32x32x16_bf16 %1, v[156:159], v[204:207], %1\n"
    "v_cvt_pk_bf16_f32 v195, v166, v167\n"
    "v_exp_f32 v170, v170\n"
    "v_exp_f32 v171, v171\n"
    "v_cvt_pk_bf16_f32 v196, v168, v169\n"
    "v_mfma_f32_16x16x32_bf16 %3, %8, v[204:207], %3\n"
    "v_exp_f32 v172, v172\n"
    "v_exp_f32 v173, v173\n"
    "v_cvt_pk_bf16_f32 v197, v170, v171\n"
    "v_exp_f32 v174, v174\n"
    "v_exp_f32 v175, v175\n"
    "v_cvt_pk_bf16_f32 v198, v172, v173\n"
    "v_cvt_pk_bf16_f32 v199, v174, v175\n"
    "s_waitcnt lgkmcnt(0)\n"
    "v_mfma_f32_32x32x16_bf16 v[160:175], v[136:139], %4, v[208:223]\n"
    "v_exp_f32 v176, v176\n"
    "v_exp_f32 v177, v177\n"
    "v_exp_f32 v178, v178\n"
    "v_mfma_f32_32x32x16_bf16 v[160:175], v[140:143], %5, v[160:175]\n"
    "v_exp_f32 v179, v179\n"
    "v_cvt_pk_bf16_f32 v200, v176, v177\n"
    "v_exp_f32 v180, v180\n"
    "v_exp_f32 v181, v181\n"
    "v_mfma_f32_32x32x16_bf16 %0, v[144:147], v[192:195], %0\n"
    "v_cvt_pk_bf16_f32 v201, v178, v179\n"
    "v_exp_f32 v182, v182\n"
    "v_exp_f32 v183, v183\n"
    "v_cvt_pk_bf16_f32 v202, v180, v181\n"
    "v_mfma_f32_16x16x32_bf16 %2, %8, v[192:195], %2\n"
    "v_exp_f32 v184, v184\n"
    "v_exp_f32 v185, v185\n"
    "v_mfma_f32_32x32x16_bf16 %0, v[148:151], v[196:199], %0\n"
    "v_cvt_pk_bf16_f32 v203, v182, v183\n"
    "v_exp_f32 v186, v186\n"
    "v_exp_f32 v187, v187\n"
    "v_cvt_pk_bf16_f32 v204, v184, v185\n"
    "v_mfma_f32_16x16x32_bf16 %2, %8, v[196:199], %2\n"
    "v_exp_f32 v188, v188\n"
    "v_exp_f32 v189, v189\n"
    "v_cvt_pk_bf16_f32 v205, v186, v187\n"
    "v_exp_f32 v190, v190\n"
    "v_exp_f32 v191, v191\n"
    "v_cvt_pk_bf16_f32 v206, v188, v189\n"
    "v_cvt_pk_bf16_f32 v207, v190, v191\n"
    "s_waitcnt vmcnt(4)\n"
    "s_barrier\n"
    "v_mfma_f32_32x32x16_bf16 v[176:191], v[136:139], %6, v[224:239]\n"
    "ds_read_b128 v[128:131], v240 offset:32768\n"
    "v_exp_f32 v160, v160\n"
    "v_exp_f32 v161, v161\n"
    "v_exp_f32 v162, v162\n"
    "ds_read_b128 v[132:135], v240 offset:33792\n"
    "v_mfma_f32_32x32x16_bf16 v[176:191], v[140:143], %7, v[176:191]\n"
    "v_exp_f32 v163, v163\n"
    "v_cvt_pk_bf16_f32 v192, v160, v161\n"
    "ds_read_b128 v[152:155], v240 offset:14336\n"
    "v_exp_f32 v164, v164\n"
    "v_exp_f32 v165, v165\n"
    "ds_read_b128 v[156:159], v240 offset:15360\n"
    "v_mfma_f32_32x32x16_bf16 %1, v[144:147], v[200:203], %1\n"
    "v_cvt_pk_bf16_f32 v193, v162, v163\n"
    "v_exp_f32 v166, v166\n"
    "v_exp_f32 v167, v167\n"
    "v_cvt_pk_bf16_f32 v194, v164, v165\n"
    "v_mfma_f32_16x16x32_bf16 %3, %8, v[200:203], %3\n"
    "v_exp_f32 v168, v168\n"
    "v_exp_f32 v169, v169\n"
    "v_mfma_f32_32x32x16_bf16 %1, v[148:151], v[204:207], %1\n"
    "v_cvt_pk_bf16_f32 v195, v166, v167\n"
    "v_exp_f32 v170, v170\n"
    "v_exp_f32 v171, v171\n"
    "v_cvt_pk_bf16_f32 v196, v168, v169\n"
    "v_mfma_f32_16x16x32_bf16 %3, %8, v[204:207], %3\n"
    "v_exp_f32 v172, v172\n"
    "v_exp_f32 v173, v173\n"
    "v_cvt_pk_bf16_f32 v197, v170, v171\n"
    "v_exp_f32 v174, v174\n"
    "v_exp_f32 v175, v175\n"
    "v_cvt_pk_bf16_f32 v198, v172, v173\n"
    "v_cvt_pk_bf16_f32 v199, v174, v175\n"
    "s_waitcnt lgkmcnt(0)\n"
    "v_mfma_f32_32x32x16_bf16 v[160:175], v[128:131], %4, v[208:223]\n"
    "v_exp_f32 v176, v176\n"
    "v_exp_f32 v177, v177\n"
    "v_exp_f32 v178, v178\n"
    "v_mfma_f32_32x32x16_bf16 v[160:175], v[132:135], %5, v[160:175]\n"
    "v_exp_f32 v179, v179\n"
    "v_cvt_pk_bf16_f32 v200, v176, v177\n"
    "v_exp_f32 v180, v180\n"
    "v_exp_f32 v181, v181\n"
    "v_mfma_f32_32x32x16_bf16 %0, v[152:155], v[192:195], %0\n"
    "v_cvt_pk_bf16_f32 v201, v178, v179\n"
    "v_exp_f32 v182, v182\n"
    "v_exp_f32 v183, v183\n"
    "v_cvt_pk_bf16_f32 v202, v180, v181\n"
    "v_mfma_f32_16x16x32_bf16 %2, %8, v[192:195], %2\n"
    "v_exp_f32 v184, v184\n"
    "v_exp_f32 v185, v185\n"
    "v_mfma_f32_32x32x16_bf16 %0, v[156:159], v[196:199], %0\n"
    "v_cvt_pk_bf16_f32 v203, v182, v183\n"
    "v_exp_f32 v186, v186\n"
    "v_exp_f32 v187, v187\n"
    "v_cvt_pk_bf16_f32 v204, v184, v185\n"
    "v_mfma_f32_16x16x32_bf16 %2, %8, v[196:199], %2\n"
    "v_exp_f32 v188, v188\n"
    "v_exp_f32 v189, v189\n"
    "v_cvt_pk_bf16_f32 v205, v186, v187\n"
    "v_exp_f32 v190, v190\n"
    "v_exp_f32 v191, v191\n"
    "v_cvt_pk_bf16_f32 v206, v188, v189\n"
    "v_cvt_pk_bf16_f32 v207, v190, v191\n"
    "v_mfma_f32_32x32x16_bf16 v[176:191], v[128:131], %6, v[224:239]\n"
    "ds_read_b128 v[136:139], v240 offset:36864\n"
    "v_exp_f32 v160, v160\n"
    "v_exp_f32 v161, v161\n"
    "v_exp_f32 v162, v162\n"
    "ds_read_b128 v[140:143], v240 offset:37888\n"
    "v_mfma_f32_32x32x16_bf16 v[176:191], v[132:135], %7, v[176:191]\n"
    "v_exp_f32 v163, v163\n"
    "v_cvt_pk_bf16_f32 v192, v160, v161\n"
    "ds_read_b128 v[144:147], v240 offset:34816\n"
    "v_exp_f32 v164, v164\n"
    "v_exp_f32 v165, v165\n"
    "ds_read_b128 v[148:151], v240 offset:35840\n"
    "v_mfma_f32_32x32x16_bf16 %1, v[152:155], v[200:203], %1\n"
    "v_cvt_pk_bf16_f32 v193, v162, v163\n"
    "v_exp_f32 v166, v166\n"
    "v_exp_f32 v167, v167\n"
    "v_cvt_pk_bf16_f32 v194, v164, v165\n"
    "v_mfma_f32_16x16x32_bf16 %3, %8, v[200:203], %3\n"
    "v_exp_f32 v168, v168\n"
    "v_exp_f32 v169, v169\n"
    "v_mfma_f32_32x32x16_bf16 %1, v[156:159], v[204:207], %1\n"
    "v_cvt_pk_bf16_f32 v195, v166, v167\n"
    "v_exp_f32 v170, v170\n"
    "v_exp_f32 v171, v171\n"
    "v_cvt_pk_bf16_f32 v196, v168, v169\n"
    "v_mfma_f32_16x16x32_bf16 %3, %8, v[204:207], %3\n"
    "v_exp_f32 v172, v172\n"
    "v_exp_f32 v173, v173\n"
    "v_cvt_pk_bf16_f32 v197, v170, v171\n"
    "v_exp_f32 v174, v174\n"
    "v_exp_f32 v175, v175\n"
    "v_cvt_pk_bf16_f32 v198, v172, v173\n"
    "v_cvt_pk_bf16_f32 v199, v174, v175\n"
    "s_waitcnt lgkmcnt(0)\n"
    "v_mfma_f32_32x32x16_bf16 v[160:175], v[136:139], %4, v[208:223]\n"
    "v_exp_f32 v176, v176\n"
    "v_exp_f32 v177, v177\n"
    "v_exp_f32 v178, v178\n"
    "v_mfma_f32_32x32x16_bf16 v[160:175], v[140:143], %5, v[160:175]\n"
    "v_exp_f32 v179, v179\n"
    "v_cvt_pk_bf16_f32 v200, v176, v177\n"
    "v_exp_f32 v180, v180\n"
    "v_exp_f32 v181, v181\n"
    "v_mfma_f32_32x32x16_bf16 %0, v[144:147], v[192:195], %0\n"
    "v_cvt_pk_bf16_f32 v201, v178, v179\n"
    "v_exp_f32 v182, v182\n"
    "v_exp_f32 v183, v183\n"
    "v_cvt_pk_bf16_f32 v202, v180, v181\n"
    "v_mfma_f32_16x16x32_bf16 %2, %8, v[192:195], %2\n"
    "v_exp_f32 v184, v184\n"
    "v_exp_f32 v185, v185\n"
    "v_mfma_f32_32x32x16_bf16 %0, v[148:151], v[196:199], %0\n"
    "v_cvt_pk_bf16_f32 v203, v182, v183\n"
    "v_exp_f32 v186, v186\n"
    "v_exp_f32 v187, v187\n"
    "v_cvt_pk_bf16_f32 v204, v184, v185\n"
    "v_mfma_f32_16x16x32_bf16 %2, %8, v[196:199], %2\n"
    "v_exp_f32 v188, v188\n"
    "v_exp_f32 v189, v189\n"
    "v_cvt_pk_bf16_f32 v205, v186, v187\n"
    "v_exp_f32 v190, v190\n"
    "v_exp_f32 v191, v191\n"
    "v_cvt_pk_bf16_f32 v206, v188, v189\n"
    "v_cvt_pk_bf16_f32 v207, v190, v191\n"
    "v_mfma_f32_32x32x16_bf16 v[176:191], v[136:139], %6, v[224:239]\n"
    "ds_read_b128 v[128:131], v240 offset:40960\n"
    "v_exp_f32 v160, v160\n"
    "v_exp_f32 v161, v161\n"
    "v_exp_f32 v162, v162\n"
    "ds_read_b128 v[132:135], v240 offset:41984\n"
    "v_mfma_f32_32x32x16_bf16 v[176:191], v[140:143], %7, v[176:191]\n"
    "v_exp_f32 v163, v163\n"
    "v_cvt_pk_bf16_f32 v192, v160, v161\n"
    "ds_read_b128 v[152:155], v240 offset:38912\n"
    "v_exp_f32 v164, v164\n"
    "v_exp_f32 v165, v165\n"
    "ds_read_b128 v[156:159], v240 offset:39936\n"
    "v_mfma_f32_32x32x16_bf16 %1, v[144:147], v[200:203], %1\n"
    "v_cvt_pk_bf16_f32 v193, v162, v163\n"
    "v_exp_f32 v166, v166\n"
    "v_exp_f32 v167, v167\n"
    "v_cvt_pk_bf16_f32 v194, v164, v165\n"
    "v_mfma_f32_16x16x32_bf16 %3, %8, v[200:203], %3\n"
    "v_exp_f32 v168, v168\n"
    "v_exp_f32 v169, v169\n"
    "v_mfma_f32_32x32x16_bf16 %1, v[148:151], v[204:207], %1\n"
    "v_cvt_pk_bf16_f32 v195, v166, v167\n"
    "v_exp_f32 v170, v170\n"
    "v_exp_f32 v171, v171\n"
    "v_cvt_pk_bf16_f32 v196, v168, v169\n"
    "v_mfma_f32_16x16x32_bf16 %3, %8, v[204:207], %3\n"
    "v_exp_f32 v172, v172\n"
    "v_exp_f32 v173, v173\n"
    "v_cvt_pk_bf16_f32 v197, v170, v171\n"
    "v_exp_f32 v174, v174\n"
    "v_exp_f32 v175, v175\n"
    "v_cvt_pk_bf16_f32 v198, v172, v173\n"
    "v_cvt_pk_bf16_f32 v199, v174, v175\n"
    "s_waitcnt lgkmcnt(0)\n"
    "v_mfma_f32_32x32x16_bf16 v[160:175], v[128:131], %4, v[208:223]\n"
    "v_exp_f32 v176, v176\n"
    "v_exp_f32 v177, v177\n"
    "v_exp_f32 v178, v178\n"
    "v_mfma_f32_32x32x16_bf16 v[160:175], v[132:135], %5, v[160:175]\n"
    "v_exp_f32 v179, v179\n"
    "v_cvt_pk_bf16_f32 v200, v176, v177\n"
    "v_exp_f32 v180, v180\n"
    "v_exp_f32 v181, v181\n"
    "v_mfma_f32_32x32x16_bf16 %0, v[152:155], v[192:195], %0\n"
    "v_cvt_pk_bf16_f32 v201, v178, v179\n"
    "v_exp_f32 v182, v182\n"
    "v_exp_f32 v183, v183\n"
    "v_cvt_pk_bf16_f32 v202, v180, v181\n"
    "v_mfma_f32_16x16x32_bf16 %2, %8, v[192:195], %2\n"
    "v_exp_f32 v184, v184\n"
    "v_exp_f32 v185, v185\n"
    "v_mfma_f32_32x32x16_bf16 %0, v[156:159], v[196:199], %0\n"
    "v_cvt_pk_bf16_f32 v203, v182, v183\n"
    "v_exp_f32 v186, v186\n"
    "v_exp_f32 v187, v187\n"
    "v_cvt_pk_bf16_f32 v204, v184, v185\n"
    "v_mfma_f32_16x16x32_bf16 %2, %8, v[196:199], %2\n"
    "v_exp_f32 v188, v188\n"
    "v_exp_f32 v189, v189\n"
    "v_cvt_pk_bf16_f32 v205, v186, v187\n"
    "v_exp_f32 v190, v190\n"
    "v_exp_f32 v191, v191\n"
    "v_cvt_pk_bf16_f32 v206, v188, v189\n"
    "v_cvt_pk_bf16_f32 v207, v190, v191\n"
    "v_mfma_f32_32x32x16_bf16 v[176:191], v[128:131], %6, v[224:239]\n"
    "ds_read_b128 v[136:139], v240 offset:45056\n"
    "v_exp_f32 v160, v160\n"
    "v_exp_f32 v161, v161\n"
    "v_exp_f32 v162, v162\n"
    "ds_read_b128 v[140:143], v240 offset:46080\n"
    "v_mfma_f32_32x32x16_bf16 v[176:191], v[132:135], %7, v[176:191]\n"
    "v_exp_f32 v163, v163\n"
    "v_cvt_pk_bf16_f32 v192, v160, v161\n"
    "ds_read_b128 v[144:147], v240 offset:43008\n"
    "v_exp_f32 v164, v164\n"
    "v_exp_f32 v165, v165\n"
    "ds_read_b128 v[148:151], v240 offset:44032\n"
    "v_mfma_f32_32x32x16_bf16 %1, v[152:155], v[200:203], %1\n"
    "v_cvt_pk_bf16_f32 v193, v162, v163\n"
    "v_exp_f32 v166, v166\n"
    "v_exp_f32 v167, v167\n"
    "v_cvt_pk_bf16_f32 v194, v164, v165\n"
    "v_mfma_f32_16x16x32_bf16 %3, %8, v[200:203], %3\n"
    "v_exp_f32 v168, v168\n"
    "v_exp_f32 v169, v169\n"
    "v_mfma_f32_32x32x16_bf16 %1, v[156:159], v[204:207], %1\n"
    "v_cvt_pk_bf16_f32 v195, v166, v167\n"
    "v_exp_f32 v170, v170\n"
    "v_exp_f32 v171, v171\n"
    "v_cvt_pk_bf16_f32 v196, v168, v169\n"
    "v_mfma_f32_16x16x32_bf16 %3, %8, v[204:207], %3\n"
    "v_exp_f32 v172, v172\n"
    "v_exp_f32 v173, v173\n"
    "v_cvt_pk_bf16_f32 v197, v170, v171\n"
    "v_exp_f32 v174, v174\n"
    "v_exp_f32 v175, v175\n"
    "v_cvt_pk_bf16_f32 v198, v172, v173\n"
    "v_cvt_pk_bf16_f32 v199, v174, v175\n"
    "s_waitcnt lgkmcnt(0)\n"
    "v_mfma_f32_32x32x16_bf16 v[160:175], v[136:139], %4, v[208:223]\n"
    "v_exp_f32 v176, v176\n"
    "v_exp_f32 v177, v177\n"
    "v_exp_f32 v178, v178\n"
    "v_mfma_f32_32x32x16_bf16 v[160:175], v[140:143], %5, v[160:175]\n"
    "v_exp_f32 v179, v179\n"
    "v_cvt_pk_bf16_f32 v200, v176, v177\n"
    "v_exp_f32 v180, v180\n"
    "v_exp_f32 v181, v181\n"
    "v_mfma_f32_32x32x16_bf16 %0, v[144:147], v[192:195], %0\n"
    "v_cvt_pk_bf16_f32 v201, v178, v179\n"
    "v_exp_f32 v182, v182\n"
    "v_exp_f32 v183, v183\n"
    "v_cvt_pk_bf16_f32 v202, v180, v181\n"
    "v_mfma_f32_16x16x32_bf16 %2, %8, v[192:195], %2\n"
    "v_exp_f32 v184, v184\n"
    "v_exp_f32 v185, v185\n"
    "v_mfma_f32_32x32x16_bf16 %0, v[148:151], v[196:199], %0\n"
    "v_cvt_pk_bf16_f32 v203, v182, v183\n"
    "v_exp_f32 v186, v186\n"
    "v_exp_f32 v187, v187\n"
    "v_cvt_pk_bf16_f32 v204, v184, v185\n"
    "v_mfma_f32_16x16x32_bf16 %2, %8, v[196:199], %2\n"
    "v_exp_f32 v188, v188\n"
    "v_exp_f32 v189, v189\n"
    "v_cvt_pk_bf16_f32 v205, v186, v187\n"
    "v_exp_f32 v190, v190\n"
    "v_exp_f32 v191, v191\n"
    "v_cvt_pk_bf16_f32 v206, v188, v189\n"
    "v_cvt_pk_bf16_f32 v207, v190, v191\n"
    "v_mfma_f32_32x32x16_bf16 v[176:191], v[136:139], %6, v[224:239]\n"
    "ds_read_b128 v[128:131], %9 offset:0\n"
    "v_exp_f32 v160, v160\n"
    "v_exp_f32 v161, v161\n"
    "v_exp_f32 v162, v162\n"
    "ds_read_b128 v[132:135], %9 offset:1024\n"
    "v_mfma_f32_32x32x16_bf16 v[176:191], v[140:143], %7, v[176:191]\n"
    "v_exp_f32 v163, v163\n"
    "v_cvt_pk_bf16_f32 v192, v160, v161\n"
    "ds_read_b128 v[152:155], v240 offset:47104\n"
    "v_exp_f32 v164, v164\n"
    "v_exp_f32 v165, v165\n"
    "ds_read_b128 v[156:159], v240 offset:48128\n"
    "v_mfma_f32_32x32x16_bf16 %1, v[144:147], v[200:203], %1\n"
    "v_cvt_pk_bf16_f32 v193, v162, v163\n"
    "v_exp_f32 v166, v166\n"
    "v_exp_f32 v167, v167\n"
    "v_cvt_pk_bf16_f32 v194, v164, v165\n"
    "v_mfma_f32_16x16x32_bf16 %3, %8, v[200:203], %3\n"
    "v_exp_f32 v168, v168\n"
    "v_exp_f32 v169, v169\n"
    "v_mfma_f32_32x32x16_bf16 %1, v[148:151], v[204:207], %1\n"
    "v_cvt_pk_bf16_f32 v195, v166, v167\n"
    "v_exp_f32 v170, v170\n"
    "v_exp_f32 v171, v171\n"
    "v_cvt_pk_bf16_f32 v196, v168, v169\n"
    "v_mfma_f32_16x16x32_bf16 %3, %8, v[204:207], %3\n"
    "v_exp_f32 v172, v172\n"
    "v_exp_f32 v173, v173\n"
    "v_cvt_pk_bf16_f32 v197, v170, v171\n"
    "v_exp_f32 v174, v174\n"
    "v_exp_f32 v175, v175\n"
    "v_cvt_pk_bf16_f32 v198, v172, v173\n"
    "v_cvt_pk_bf16_f32 v199, v174, v175\n"
    "s_waitcnt lgkmcnt(0)\n"
    "v_mfma_f32_32x32x16_bf16 v[160:175], v[128:131], %4, v[208:223]\n"
    "v_exp_f32 v176, v176\n"
    "v_exp_f32 v177, v177\n"
    "v_exp_f32 v178, v178\n"
    "v_mfma_f32_32x32x16_bf16 v[160:175], v[132:135], %5, v[160:175]\n"
    "v_exp_f32 v179, v179\n"
    "v_cvt_pk_bf16_f32 v200, v176, v177\n"
    "v_exp_f32 v180, v180\n"
    "v_exp_f32 v181, v181\n"
    "v_mfma_f32_32x32x16_bf16 %0, v[152:155], v[192:195], %0\n"
    "v_cvt_pk_bf16_f32 v201, v178, v179\n"
    "v_exp_f32 v182, v182\n"
    "v_exp_f32 v183, v183\n"
    "v_cvt_pk_bf16_f32 v202, v180, v181\n"
    "v_mfma_f32_16x16x32_bf16 %2, %8, v[192:195], %2\n"
    "v_exp_f32 v184, v184\n"
    "v_exp_f32 v185, v185\n"
    "v_mfma_f32_32x32x16_bf16 %0, v[156:159], v[196:199], %0\n"
    "v_cvt_pk_bf16_f32 v203, v182, v183\n"
    "v_exp_f32 v186, v186\n"
    "v_exp_f32 v187, v187\n"
    "v_cvt_pk_bf16_f32 v204, v184, v185\n"
    "v_mfma_f32_16x16x32_bf16 %2, %8, v[196:199], %2\n"
    "v_exp_f32 v188, v188\n"
    "v_exp_f32 v189, v189\n"
    "v_cvt_pk_bf16_f32 v205, v186, v187\n"
    "v_exp_f32 v190, v190\n"
    "v_exp_f32 v191, v191\n"
    "v_cvt_pk_bf16_f32 v206, v188, v189\n"
    "v_cvt_pk_bf16_f32 v207, v190, v191\n"
    "s_nop 1\n"
    "v_mfma_f32_32x32x16_bf16 %1, v[152:155], v[200:203], %1\n"
    "v_mfma_f32_32x32x16_bf16 %1, v[156:159], v[204:207], %1\n"
    "v_mfma_f32_16x16x32_bf16 %3, %8, v[200:203], %3\n"
    "v_mfma_f32_16x16x32_bf16 %3, %8, v[204:207], %3\n"
    "s_waitcnt vmcnt(0)\n"
    "s_nop 15\n"
        : "+v"(o[0]), "+v"(o[1]), "+v"(l[0]), "+v"(l[1])
        : "v"(qf[0][0]), "v"(qf[0][1]), "v"(qf[1][0]), "v"(qf[1][1]), "v"(ones), "v"(lb), "v"(goff), "s"(kvr), "s"(ldsw), "s"(soff0), "v"(myflag), "v"(pollp), "v"(tag), "v"(wov), "s"(wor)
        : "v128", "v129", "v130", "v131", "v132", "v133", "v134", "v135", "v136", "v137", "v138", "v139", "v140", "v141", "v142", "v143", "v144", "v145", "v146", "v147", "v148", "v149", "v150", "v151", "v152", "v153", "v154", "v155", "v156", "v157", "v158", "v159", "v160", "v161", "v162", "v163", "v164", "v165", "v166", "v167", "v168", "v169", "v170", "v171", "v172", "v173", "v174", "v175", "v176", "v177", "v178", "v179", "v180", "v181", "v182", "v183", "v184", "v185", "v186", "v187", "v188", "v189", "v190", "v191", "v192", "v193", "v194", "v195", "v196", "v197", "v198", "v199", "v200", "v201", "v202", "v203", "v204", "v205", "v206", "v207", "v208", "v209", "v210", "v211", "v212", "v213", "v214", "v215", "v216", "v217", "v218", "v219", "v220", "v221", "v222", "v223", "v224", "v225", "v226", "v227", "v228", "v229", "v230", "v231", "v232", "v233", "v234", "v235", "v236", "v237", "v238", "v239", "v240", "v241", "v242", "s40", "s41", "s42", "s43", "s44", "s45", "s46", "s47", "s48", "s49", "s50", "s51", "s52", "s53", "s54", "s55", "s56", "s57", "vcc", "scc", "memory");
    __builtin_amdgcn_s_setprio(0);
    const __amdgpu_buffer_rsrc_t ow = __builtin_amdgcn_make_buffer_rsrc((void*)(ws + WS_O + (((size_t)b * 64 + rb * 8 + wm * 2) * 64 + 2 * h) * 1024), (short)0, 2 * 65536, 0x00020000);
#pragma unroll
    for (int i = 0; i < 2; ++i) {
      float lsum = l[i][0];
      float amax = 0.f;
#pragma unroll
      for (int r = 0; r < 16; ++r) amax = fmaxf(amax, fabsf(o[i][r]));
      if (__builtin_expect(__any(!(lsum < 1.0e30f) || !(amax < 1.0e37f)), 0)) {
        float m = -INFINITY; lsum = 0.f; f32x16 oo = {};
        const int lf = lane_id();
        const __amdgpu_buffer_rsrc_t kr = __builtin_amdgcn_make_buffer_rsrc((void*)kvh, (short)0, 64 * 4096, 0x00020000);
        for (int kb = 0; kb < 64; ++kb) {
          const unsigned so = kb * 4096 + lf * 16;
          const bf16x8 k0 = __builtin_bit_cast(bf16x8, __builtin_amdgcn_raw_buffer_load_b128(kr, so, 0, AUX_SC1)), k1 = __builtin_bit_cast(bf16x8, __builtin_amdgcn_raw_buffer_load_b128(kr, so + 1024, 0, AUX_SC1));
          const bf16x8 v0 = __builtin_bit_cast(bf16x8, __builtin_amdgcn_raw_buffer_load_b128(kr, so + 2048, 0, AUX_SC1)), v1 = __builtin_bit_cast(bf16x8, __builtin_amdgcn_raw_buffer_load_b128(kr, so + 3072, 0, AUX_SC1));
          f32x16 z = {}; f32x16 s = MFMA32(k0, qf[i][0], z); s = MFMA32(k1, qf[i][1], s);
          float mx = s[0];
#pragma unroll
          for (int r = 1; r < 16; ++r) mx = fmaxf(mx, s[r]);
          mx = fmaxf(mx, __shfl_xor(mx, 32));
          const float mn = fmaxf(m, mx); const float al = __builtin_amdgcn_exp2f(m - mn); m = mn;
          float rs = 0.f;
#pragma unroll
          for (int r = 0; r < 16; ++r) s[r] = __builtin_amdgcn_exp2f(s[r] - mn);
          const u32x4 w0 = pack8(s, 0), w1 = pack8(s, 1);
#pragma unroll
          for (int r = 0; r < 4; ++r) { rs += __uint_as_float(w0[r] << 16) + __uint_as_float(w0[r] & 0xffff0000u); rs += __uint_as_float(w1[r] << 16) + __uint_as_float(w1[r] & 0xffff0000u); }
          rs += __shfl_xor(rs, 32);
          lsum = lsum * al + rs;
          oo = oo * al;
          oo = MFMA32(v0, __builtin_bit_cast(bf16x8, w0), oo); oo = MFMA32(v1, __builtin_bit_cast(bf16x8, w1), oo);
        }
        o[i] = oo;
      }
      const float inv = 1.0f / lsum;
      const f32x16 on = o[i] * inv;
      const int lo_ = lane_id();
      __builtin_amdgcn_raw_buffer_store_b128(pack8(on, 0), ow, i * 65536 + lo_ * 16, 0, AUX_SC1);
      __builtin_amdgcn_raw_buffer_store_b128(pack8(on, 1), ow, i * 65536 + 1024 + lo_ * 16, 0, AUX_SC1);
    }
    const int rb2 = 2 * rb + (hp >> 3), cb = hp & 7;
    const int tm = (wid >> 1) & 1, cn = wid & 1, lw4 = wid & 3;
    const int ln = lane_id();
    GAS unsigned* fo = (GAS unsigned*)(ws + WS_FO) + (((b * 8 + rb) * 4 + wm) * 16 + hp) * 2 + wn;
    GAS unsigned* opoll = (GAS unsigned*)(ws + WS_FO) + ((b * 8 + rb) * 4 + 2 * (rb2 & 1)) * 32 + ln;
    const __amdgpu_buffer_rsrc_t orr = __builtin_amdgcn_make_buffer_rsrc((void*)(ws + WS_O + ((size_t)b * 64 + rb2 * 4) * 65536), (short)0, 4 * 65536, 0x00020000);
    unsigned woff[2];
#pragma unroll
    for (int c = 0; c < 2; ++c) { int g = ln >> 4, i = ln & 15, q = i >> 2, p = i & 3; int k = 4 * (g >> 1) + q;
      int cs = 32 * c + 16 * (g & 1) + 8 * (p & 1) + 4 * (p >> 1), ch = (cs >> 3) ^ (((k >> 1) & 1) << 2);
      woff[c] = ldsb + 16384 + cn * 8192 + k * 128 + ch * 16 + ((cs >> 2) & 1) * 8; }
    unsigned wsv[4];
#pragma unroll
    for (int i = 0; i < 4; ++i) { int k = 8 * (4 * (lw4 & 1) + i) + (ln >> 3), ch = (ln & 7) ^ (((k >> 1) & 1) << 2); wsv[i] = k * 2048 + (lw4 >> 1) * 128 + ch * 16 - i * 1024; }
    const unsigned os = lw4 * 65536 + ln * 16;
    const unsigned ooff0 = ldsb + tm * 8192 + ln * 16;
    const unsigned lo = __builtin_amdgcn_readfirstlane(ldsb + lw4 * 4096), lw = __builtin_amdgcn_readfirstlane(ldsb + 16384 + lw4 * 4096);

    f32x16 c00 = {}, c01 = {}, c10 = {}, c11 = {};
    asm volatile(
    "s_nop 4\n"
    "s_waitcnt vmcnt(0)\n"
    "global_store_dword %19, %21, off sc1\n"
    "s_barrier\n"
    "s_cmp_lt_u32 %22, 4\n"
    "s_cbranch_scc1 Lcomp_%=\n"
    "s_mov_b32 s40, -4096\n"
    "s_mov_b32 s41, -131072\n"
    "s_add_u32 m0, %18, 0\n"
    "s_add_u32 s41, s41, 131072\n"
    "buffer_load_dwordx4 %10, %16, s41 offen offset:0 lds\n"
    "buffer_load_dwordx4 %11, %16, s41 offen offset:1024 lds\n"
    "buffer_load_dwordx4 %12, %16, s41 offen offset:2048 lds\n"
    "buffer_load_dwordx4 %13, %16, s41 offen offset:3072 lds\n"
    "s_add_u32 m0, %18, 32768\n"
    "s_add_u32 s41, s41, 131072\n"
    "buffer_load_dwordx4 %10, %16, s41 offen offset:0 lds\n"
    "buffer_load_dwordx4 %11, %16, s41 offen offset:1024 lds\n"
    "buffer_load_dwordx4 %12, %16, s41 offen offset:2048 lds\n"
    "buffer_load_dwordx4 %13, %16, s41 offen offset:3072 lds\n"
    "s_add_u32 m0, %18, 65536\n"
    "s_add_u32 s41, s41, 131072\n"
    "buffer_load_dwordx4 %10, %16, s41 offen offset:0 lds\n"
    "buffer_load_dwordx4 %11, %16, s41 offen offset:1024 lds\n"
    "buffer_load_dwordx4 %12, %16, s41 offen offset:2048 lds\n"
    "buffer_load_dwordx4 %13, %16, s41 offen offset:3072 lds\n"
    "s_add_u32 m0, %18, 98304\n"
    "s_add_u32 s41, s41, 131072\n"
    "buffer_load_dwordx4 %10, %16, s41 offen offset:0 lds\n"
    "buffer_load_dwordx4 %11, %16, s41 offen offset:1024 lds\n"
    "buffer_load_dwordx4 %12, %16, s41 offen offset:2048 lds\n"
    "buffer_load_dwordx4 %13, %16, s41 offen offset:3072 lds\n"
    "s_mov_b32 s53, 0\n"
    "Lopoll_%=:\n"
    "global_load_dword v96, %20, off sc1\n"
    "s_waitcnt vmcnt(0)\n"
    "v_cmp_ne_u32 vcc, v96, %21\n"
    "s_nop 4\n"
    "s_cbranch_vccz Loready_%=\n"
    "s_sleep 1\n"
    "s_add_u32 s53, s53, 1\n"
    "s_cmp_lt_u32 s53, 200000\n"
    "s_cbranch_scc1 Lopoll_%=\n"
    "Loready_%=:\n"
    "s_add_u32 m0, %17, 0\n"
    "s_add_u32 s40, s40, 4096\n"
    "buffer_load_dwordx4 %14, %15, s40 offen offset:0 sc1 lds\n"
    "buffer_load_dwordx4 %14, %15, s40 offen offset:1024 sc1 lds\n"
    "buffer_load_dwordx4 %14, %15, s40 offen offset:2048 sc1 lds\n"
    "buffer_load_dwordx4 %14, %15, s40 offen offset:3072 sc1 lds\n"
    "s_add_u32 m0, %17, 32768\n"
    "s_add_u32 s40, s40, 4096\n"
    "buffer_load_dwordx4 %14, %15, s40 offen offset:0 sc1 lds\n"
    "buffer_load_dwordx4 %14, %15, s40 offen offset:1024 sc1 lds\n"
    "buffer_load_dwordx4 %14, %15, s40 offen offset:2048 sc1 lds\n"
    "buffer_load_dwordx4 %14, %15, s40 offen offset:3072 sc1 lds\n"
    "s_add_u32 m0, %17, 65536\n"
    "s_add_u32 s40, s40, 4096\n"
    "buffer_load_dwordx4 %14, %15, s40 offen offset:0 sc1 lds\n"
    "buffer_load_dwordx4 %14, %15, s40 offen offset:1024 sc1 lds\n"
    "buffer_load_dwordx4 %14, %15, s40 offen offset:2048 sc1 lds\n"
    "buffer_load_dwordx4 %14, %15, s40 offen offset:3072 sc1 lds\n"
    "s_add_u32 m0, %17, 98304\n"
    "s_add_u32 s40, s40, 4096\n"
    "buffer_load_dwordx4 %14, %15, s40 offen offset:0 sc1 lds\n"
    "buffer_load_dwordx4 %14, %15, s40 offen offset:1024 sc1 lds\n"
    "buffer_load_dwordx4 %14, %15, s40 offen offset:2048 sc1 lds\n"
    "buffer_load_dwordx4 %14, %15, s40 offen offset:3072 sc1 lds\n"
    "s_waitcnt vmcnt(12)\n"
    "s_barrier\n"
    "s_waitcnt vmcnt(8)\n"
    "s_barrier\n"
    "s_add_u32 m0, %17, 0\n"
    "s_add_u32 s40, s40, 4096\n"
    "buffer_load_dwordx4 %14, %15, s40 offen offset:0 sc1 lds\n"
    "buffer_load_dwordx4 %14, %15, s40 offen offset:1024 sc1 lds\n"
    "buffer_load_dwordx4 %14, %15, s40 offen offset:2048 sc1 lds\n"
    "buffer_load_dwordx4 %14, %15, s40 offen offset:3072 sc1 lds\n"
    "s_add_u32 m0, %18, 0\n"
    "s_add_u32 s41, s41, 131072\n"
    "buffer_load_dwordx4 %10, %16, s41 offen offset:0 lds\n"
    "buffer_load_dwordx4 %11, %16, s41 offen offset:1024 lds\n"
    "buffer_load_dwordx4 %12, %16, s41 offen offset:2048 lds\n"
    "buffer_load_dwordx4 %13, %16, s41 offen offset:3072 lds\n"
    "s_waitcnt vmcnt(12)\n"
    "s_barrier\n"
    "s_add_u32 m0, %17, 32768\n"
    "s_add_u32 s40, s40, 4096\n"
    "buffer_load_dwordx4 %14, %15, s40 offen offset:0 sc1 lds\n"
    "buffer_load_dwordx4 %14, %15, s40 offen offset:1024 sc1 lds\n"
    "buffer_load_dwordx4 %14, %15, s40 offen offset:2048 sc1 lds\n"
    "buffer_load_dwordx4 %14, %15, s40 offen offset:3072 sc1 lds\n"
    "s_add_u32 m0, %18, 32768\n"
    "s_add_u32 s41, s41, 131072\n"
    "buffer_load_dwordx4 %10, %16, s41 offen offset:0 lds\n"
    "buffer_load_dwordx4 %11, %16, s41 offen offset:1024 lds\n"
    "buffer_load_dwordx4 %12, %16, s41 offen offset:2048 lds\n"
    "buffer_load_dwordx4 %13, %16, s41 offen offset:3072 lds\n"
    "s_waitcnt vmcnt(16)\n"
    "s_barrier\n"
    "s_add_u32 m0, %17, 65536\n"
    "s_add_u32 s40, s40, 4096\n"
    "buffer_load_dwordx4 %14, %15, s40 offen offset:0 sc1 lds\n"
    "buffer_load_dwordx4 %14, %15, s40 offen offset:1024 sc1 lds\n"
    "buffer_load_dwordx4 %14, %15, s40 offen offset:2048 sc1 lds\n"
    "buffer_load_dwordx4 %14, %15, s40 offen offset:3072 sc1 lds\n"
    "s_add_u32 m0, %18, 65536\n"
    "s_add_u32 s41, s41, 131072\n"
    "buffer_load_dwordx4 %10, %16, s41 offen offset:0 lds\n"
    "buffer_load_dwordx4 %11, %16, s41 offen offset:1024 lds\n"
    "buffer_load_dwordx4 %12, %16, s41 offen offset:2048 lds\n"
    "buffer_load_dwordx4 %13, %16, s41 offen offset:3072 lds\n"
    "s_waitcnt vmcnt(16)\n"
    "s_barrier\n"
    "s_add_u32 m0, %17, 98304\n"
    "s_add_u32 s40, s40, 4096\n"
    "buffer_load_dwordx4 %14, %15, s40 offen offset:0 sc1 lds\n"
    "buffer_load_dwordx4 %14, %15, s40 offen offset:1024 sc1 lds\n"
    "buffer_load_dwordx4 %14, %15, s40 offen offset:2048 sc1 lds\n"
    "buffer_load_dwordx4 %14, %15, s40 offen offset:3072 sc1 lds\n"
    "s_add_u32 m0, %18, 98304\n"
    "s_add_u32 s41, s41, 131072\n"
    "buffer_load_dwordx4 %10, %16, s41 offen offset:0 lds\n"
    "buffer_load_dwordx4 %11, %16, s41 offen offset:1024 lds\n"
    "buffer_load_dwordx4 %12, %16, s41 offen offset:2048 lds\n"
    "buffer_load_dwordx4 %13, %16, s41 offen offset:3072 lds\n"
    "s_waitcnt vmcnt(16)\n"
    "s_barrier\n"
    "s_add_u32 m0, %17, 0\n"
    "s_add_u32 s40, s40, 4096\n"
    "buffer_load_dwordx4 %14, %15, s40 offen offset:0 sc1 lds\n"
    "buffer_load_dwordx4 %14, %15, s40 offen offset:1024 sc1 lds\n"
    "buffer_load_dwordx4 %14, %15, s40 offen offset:2048 sc1 lds\n"
    "buffer_load_dwordx4 %14, %15, s40 offen offset:3072 sc1 lds\n"
    "s_add_u32 m0, %18, 0\n"
    "s_add_u32 s41, s41, 131072\n"
    "buffer_load_dwordx4 %10, %16, s41 offen offset:0 lds\n"
    "buffer_load_dwordx4 %11, %16, s41 offen offset:1024 lds\n"
    "buffer_load_dwordx4 %12, %16, s41 offen offset:2048 lds\n"
    "buffer_load_dwordx4 %13, %16, s41 offen offset:3072 lds\n"
    "s_waitcnt vmcnt(16)\n"
    "s_barrier\n"
    "s_add_u32 m0, %17, 32768\n"
    "s_add_u32 s40, s40, 4096\n"
    "buffer_load_dwordx4 %14, %15, s40 offen offset:0 sc1 lds\n"
    "buffer_load_dwordx4 %14, %15, s40 offen offset:1024 sc1 lds\n"
    "buffer_load_dwordx4 %14, %15, s40 offen offset:2048 sc1 lds\n"
    "buffer_load_dwordx4 %14, %15, s40 offen offset:3072 sc1 lds\n"
    "s_add_u32 m0, %18, 32768\n"
    "s_add_u32 s41, s41, 131072\n"
    "buffer_load_dwordx4 %10, %16, s41 offen offset:0 lds\n"
    "buffer_load_dwordx4 %11, %16, s41 offen offset:1024 lds\n"
    "buffer_load_dwordx4 %12, %16, s41 offen offset:2048 lds\n"
    "buffer_load_dwordx4 %13, %16, s41 offen offset:3072 lds\n"
    "s_waitcnt vmcnt(16)\n"
    "s_barrier\n"
    "s_add_u32 m0, %17, 65536\n"
    "s_add_u32 s40, s40, 4096\n"
    "buffer_load_dwordx4 %14, %15, s40 offen offset:0 sc1 lds\n"
    "buffer_load_dwordx4 %14, %15, s40 offen offset:1024 sc1 lds\n"
    "buffer_load_dwordx4 %14, %15, s40 offen offset:2048 sc1 lds\n"
    "buffer_load_dwordx4 %14, %15, s40 offen offset:3072 sc1 lds\n"
    "s_add_u32 m0, %18, 65536\n"
    "s_add_u32 s41, s41, 131072\n"
    "buffer_load_dwordx4 %10, %16, s41 offen offset:0 lds\n"
    "buffer_load_dwordx4 %11, %16, s41 offen offset:1024 lds\n"
    "buffer_load_dwordx4 %12, %16, s41 offen offset:2048 lds\n"
    "buffer_load_dwordx4 %13, %16, s41 offen offset:3072 lds\n"
    "s_waitcnt vmcnt(16)\n"
    "s_barrier\n"
    "s_add_u32 m0, %17, 98304\n"
    "s_add_u32 s40, s40, 4096\n"
    "buffer_load_dwordx4 %14, %15, s40 offen offset:0 sc1 lds\n"
    "buffer_load_dwordx4 %14, %15, s40 offen offset:1024 sc1 lds\n"
    "buffer_load_dwordx4 %14, %15, s40 offen offset:2048 sc1 lds\n"
    "buffer_load_dwordx4 %14, %15, s40 offen offset:3072 sc1 lds\n"
    "s_add_u32 m0, %18, 98304\n"
    "s_add_u32 s41, s41, 131072\n"
    "buffer_load_dwordx4 %10, %16, s41 offen offset:0 lds\n"
    "buffer_load_dwordx4 %11, %16, s41 offen offset:1024 lds\n"
    "buffer_load_dwordx4 %12, %16, s41 offen offset:2048 lds\n"
    "buffer_load_dwordx4 %13, %16, s41 offen offset:3072 lds\n"
    "s_waitcnt vmcnt(16)\n"
    "s_barrier\n"
    "s_add_u32 m0, %17, 0\n"
    "s_add_u32 s40, s40, 4096\n"
    "buffer_load_dwordx4 %14, %15, s40 offen offset:0 sc1 lds\n"
    "buffer_load_dwordx4 %14, %15, s40 offen offset:1024 sc1 lds\n"
    "buffer_load_dwordx4 %14, %15, s40 offen offset:2048 sc1 lds\n"
    "buffer_load_dwordx4 %14, %15, s40 offen offset:3072 sc1 lds\n"
    "s_add_u32 m0, %18, 0\n"
    "s_add_u32 s41, s41, 131072\n"
    "buffer_load_dwordx4 %10, %16, s41 offen offset:0 lds\n"
    "buffer_load_dwordx4 %11, %16, s41 offen offset:1024 lds\n"
    "buffer_load_dwordx4 %12, %16, s41 offen offset:2048 lds\n"
    "buffer_load_dwordx4 %13, %16, s41 offen offset:3072 lds\n"
    "s_waitcnt vmcnt(16)\n"
    "s_barrier\n"
    "s_add_u32 m0, %17, 32768\n"
    "s_add_u32 s40, s40, 4096\n"
    "buffer_load_dwordx4 %14, %15, s40 offen offset:0 sc1 lds\n"
    "buffer_load_dwordx4 %14, %15, s40 offen offset:1024 sc1 lds\n"
    "buffer_load_dwordx4 %14, %15, s40 offen offset:2048 sc1 lds\n"
    "buffer_load_dwordx4 %14, %15, s40 offen offset:3072 sc1 lds\n"
    "s_add_u32 m0, %18, 32768\n"
    "s_add_u32 s41, s41, 131072\n"
    "buffer_load_dwordx4 %10, %16, s41 offen offset:0 lds\n"
    "buffer_load_dwordx4 %11, %16, s41 offen offset:1024 lds\n"
    "buffer_load_dwordx4 %12, %16, s41 offen offset:2048 lds\n"
    "buffer_load_dwordx4 %13, %16, s41 offen offset:3072 lds\n"
    "s_waitcnt vmcnt(16)\n"
    "s_barrier\n"
    "s_add_u32 m0, %17, 65536\n"
    "s_add_u32 s40, s40, 4096\n"
    "buffer_load_dwordx4 %14, %15, s40 offen offset:0 sc1 lds\n"
    "buffer_load_dwordx4 %14, %15, s40 offen offset:1024 sc1 lds\n"
    "buffer_load_dwordx4 %14, %15, s40 offen offset:2048 sc1 lds\n"
    "buffer_load_dwordx4 %14, %15, s40 offen offset:3072 sc1 lds\n"
    "s_add_u32 m0, %18, 65536\n"
    "s_add_u32 s41, s41, 131072\n"
    "buffer_load_dwordx4 %10, %16, s41 offen offset:0 lds\n"
    "buffer_load_dwordx4 %11, %16, s41 offen offset:1024 lds\n"
    "buffer_load_dwordx4 %12, %16, s41 offen offset:2048 lds\n"
    "buffer_load_dwordx4 %13, %16, s41 offen offset:3072 lds\n"
    "s_waitcnt vmcnt(16)\n"
    "s_barrier\n"
    "s_add_u32 m0, %17, 98304\n"
    "s_add_u32 s40, s40, 4096\n"
    "buffer_load_dwordx4 %14, %15, s40 offen offset:0 sc1 lds\n"
    "buffer_load_dwordx4 %14, %15, s40 offen offset:1024 sc1 lds\n"
    "buffer_load_dwordx4 %14, %15, s40 offen offset:2048 sc1 lds\n"
    "buffer_load_dwordx4 %14, %15, s40 offen offset:3072 sc1 lds\n"
    "s_add_u32 m0, %18, 98304\n"
    "s_add_u32 s41, s41, 131072\n"
    "buffer_load_dwordx4 %10, %16, s41 offen offset:0 lds\n"
    "buffer_load_dwordx4 %11, %16, s41 offen offset:1024 lds\n"
    "buffer_load_dwordx4 %12, %16, s41 offen offset:2048 lds\n"
    "buffer_load_dwordx4 %13, %16, s41 offen offset:3072 lds\n"
    "s_waitcnt vmcnt(16)\n"
    "s_barrier\n"
    "s_waitcnt vmcnt(8)\n"
    "s_barrier\n"
    "s_waitcnt vmcnt(0)\n"
    "s_barrier\n"
    "s_branch Ldone_%=\n"
    "Lcomp_%=:\n"
    "s_barrier\n"
    "ds_read_b128 v[64:67], %4 offset:0\n"
    "ds_read_b64_tr_b16 v[72:73], %6 offset:0\n"
    "ds_read_b64_tr_b16 v[74:75], %6 offset:1024\n"
    "ds_read_b128 v[68:71], %4 offset:4096\n"
    "ds_read_b64_tr_b16 v[76:77], %7 offset:0\n"
    "ds_read_b64_tr_b16 v[78:79], %7 offset:1024\n"
    "s_mov_b32 s42, 2\n"
    "Lout_loop_%=:\n"
    "s_waitcnt lgkmcnt(0)\n"
    "v_mfma_f32_32x32x16_bf16 %0, v[72:75], v[64:67], %0\n"
    "ds_read_b128 v[80:83], %4 offset:1024\n"
    "ds_read_b64_tr_b16 v[88:89], %6 offset:2048\n"
    "v_mfma_f32_32x32x16_bf16 %2, v[72:75], v[68:71], %2\n"
    "ds_read_b64_tr_b16 v[90:91], %6 offset:3072\n"
    "ds_read_b128 v[84:87], %4 offset:5120\n"
    "v_mfma_f32_32x32x16_bf16 %1, v[76:79], v[64:67], %1\n"
    "ds_read_b64_tr_b16 v[92:93], %7 offset:2048\n"
    "ds_read_b64_tr_b16 v[94:95], %7 offset:3072\n"
    "v_mfma_f32_32x32x16_bf16 %3, v[76:79], v[68:71], %3\n"
    "s_waitcnt lgkmcnt(0)\n"
    "v_mfma_f32_32x32x16_bf16 %0, v[88:91], v[80:83], %0\n"
    "ds_read_b128 v[64:67], %4 offset:2048\n"
    "ds_read_b64_tr_b16 v[72:73], %6 offset:4096\n"
    "v_mfma_f32_32x32x16_bf16 %2, v[88:91], v[84:87], %2\n"
    "ds_read_b64_tr_b16 v[74:75], %6 offset:5120\n"
    "ds_read_b128 v[68:71], %4 offset:6144\n"
    "v_mfma_f32_32x32x16_bf16 %1, v[92:95], v[80:83], %1\n"
    "ds_read_b64_tr_b16 v[76:77], %7 offset:4096\n"
    "ds_read_b64_tr_b16 v[78:79], %7 offset:5120\n"
    "v_mfma_f32_32x32x16_bf16 %3, v[92:95], v[84:87], %3\n"
    "s_waitcnt lgkmcnt(0)\n"
    "v_mfma_f32_32x32x16_bf16 %0, v[72:75], v[64:67], %0\n"
    "ds_read_b128 v[80:83], %4 offset:3072\n"
    "ds_read_b64_tr_b16 v[88:89], %6 offset:6144\n"
    "v_mfma_f32_32x32x16_bf16 %2, v[72:75], v[68:71], %2\n"
    "ds_read_b64_tr_b16 v[90:91], %6 offset:7168\n"
    "ds_read_b128 v[84:87], %4 offset:7168\n"
    "v_mfma_f32_32x32x16_bf16 %1, v[76:79], v[64:67], %1\n"
    "ds_read_b64_tr_b16 v[92:93], %7 offset:6144\n"
    "ds_read_b64_tr_b16 v[94:95], %7 offset:7168\n"
    "v_mfma_f32_32x32x16_bf16 %3, v[76:79], v[68:71], %3\n"
    "s_waitcnt lgkmcnt(0)\n"
    "s_barrier\n"
    "v_mfma_f32_32x32x16_bf16 %0, v[88:91], v[80:83], %0\n"
    "ds_read_b128 v[64:67], %4 offset:32768\n"
    "ds_read_b64_tr_b16 v[72:73], %6 offset:32768\n"
    "v_mfma_f32_32x32x16_bf16 %2, v[88:91], v[84:87], %2\n"
    "ds_read_b64_tr_b16 v[74:75], %6 offset:33792\n"
    "ds_read_b128 v[68:71], %4 offset:36864\n"
    "v_mfma_f32_32x32x16_bf16 %1, v[92:95], v[80:83], %1\n"
    "ds_read_b64_tr_b16 v[76:77], %7 offset:32768\n"
    "ds_read_b64_tr_b16 v[78:79], %7 offset:33792\n"
    "v_mfma_f32_32x32x16_bf16 %3, v[92:95], v[84:87], %3\n"
    "s_waitcnt lgkmcnt(0)\n"
    "v_mfma_f32_32x32x16_bf16 %0, v[72:75], v[64:67], %0\n"
    "ds_read_b128 v[80:83], %4 offset:33792\n"
    "ds_read_b64_tr_b16 v[88:89], %6 offset:34816\n"
    "v_mfma_f32_32x32x16_bf16 %2, v[72:75], v[68:71], %2\n"
    "ds_read_b64_tr_b16 v[90:91], %6 offset:35840\n"
    "ds_read_b128 v[84:87], %4 offset:37888\n"
    "v_mfma_f32_32x32x16_bf16 %1, v[76:79], v[64:67], %1\n"
    "ds_read_b64_tr_b16 v[92:93], %7 offset:34816\n"
    "ds_read_b64_tr_b16 v[94:95], %7 offset:35840\n"
    "v_mfma_f32_32x32x16_bf16 %3, v[76:79], v[68:71], %3\n"
    "s_waitcnt lgkmcnt(0)\n"
    "v_mfma_f32_32x32x16_bf16 %0, v[88:91], v[80:83], %0\n"
    "ds_read_b128 v[64:67], %4 offset:34816\n"
    "ds_read_b64_tr_b16 v[72:73], %6 offset:36864\n"
    "v_mfma_f32_32x32x16_bf16 %2, v[88:91], v[84:87], %2\n"
    "ds_read_b64_tr_b16 v[74:75], %6 offset:37888\n"
    "ds_read_b128 v[68:71], %4 offset:38912\n"
    "v_mfma_f32_32x32x16_bf16 %1, v[92:95], v[80:83], %1\n"
    "ds_read_b64_tr_b16 v[76:77], %7 offset:36864\n"
    "ds_read_b64_tr_b16 v[78:79], %7 offset:37888\n"
    "v_mfma_f32_32x32x16_bf16 %3, v[92:95], v[84:87], %3\n"
    "s_waitcnt lgkmcnt(0)\n"
    "v_mfma_f32_32x32x16_bf16 %0, v[72:75], v[64:67], %0\n"
    "ds_read_b128 v[80:83], %4 offset:35840\n"
    "ds_read_b64_tr_b16 v[88:89], %6 offset:38912\n"
    "v_mfma_f32_32x32x16_bf16 %2, v[72:75], v[68:71], %2\n"
    "ds_read_b64_tr_b16 v[90:91], %6 offset:39936\n"
    "ds_read_b128 v[84:87], %4 offset:39936\n"
    "v_mfma_f32_32x32x16_bf16 %1, v[76:79], v[64:67], %1\n"
    "ds_read_b64_tr_b16 v[92:93], %7 offset:38912\n"
    "ds_read_b64_tr_b16 v[94:95], %7 offset:39936\n"
    "v_mfma_f32_32x32x16_bf16 %3, v[76:79], v[68:71], %3\n"
    "s_waitcnt lgkmcnt(0)\n"
    "s_barrier\n"
    "v_mfma_f32_32x32x16_bf16 %0, v[88:91], v[80:83], %0\n"
    "ds_read_b128 v[64:67], %5 offset:0\n"
    "ds_read_b64_tr_b16 v[72:73], %8 offset:0\n"
    "v_mfma_f32_32x32x16_bf16 %2, v[88:91], v[84:87], %2\n"
    "ds_read_b64_tr_b16 v[74:75], %8 offset:1024\n"
    "ds_read_b128 v[68:71], %5 offset:4096\n"
    "v_mfma_f32_32x32x16_bf16 %1, v[92:95], v[80:83], %1\n"
    "ds_read_b64_tr_b16 v[76:77], %9 offset:0\n"
    "ds_read_b64_tr_b16 v[78:79], %9 offset:1024\n"
    "v_mfma_f32_32x32x16_bf16 %3, v[92:95], v[84:87], %3\n"
    "s_waitcnt lgkmcnt(0)\n"
    "v_mfma_f32_32x32x16_bf16 %0, v[72:75], v[64:67], %0\n"
    "ds_read_b128 v[80:83], %5 offset:1024\n"
    "ds_read_b64_tr_b16 v[88:89], %8 offset:2048\n"
    "v_mfma_f32_32x32x16_bf16 %2, v[72:75], v[68:71], %2\n"
    "ds_read_b64_tr_b16 v[90:91], %8 offset:3072\n"
    "ds_read_b128 v[84:87], %5 offset:5120\n"
    "v_mfma_f32_32x32x16_bf16 %1, v[76:79], v[64:67], %1\n"
    "ds_read_b64_tr_b16 v[92:93], %9 offset:2048\n"
    "ds_read_b64_tr_b16 v[94:95], %9 offset:3072\n"
    "v_mfma_f32_32x32x16_bf16 %3, v[76:79], v[68:71], %3\n"
    "s_waitcnt lgkmcnt(0)\n"
    "v_mfma_f32_32x32x16_bf16 %0, v[88:91], v[80:83], %0\n"
    "ds_read_b128 v[64:67], %5 offset:2048\n"
    "ds_read_b64_tr_b16 v[72:73], %8 offset:4096\n"
    "v_mfma_f32_32x32x16_bf16 %2, v[88:91], v[84:87], %2\n"
    "ds_read_b64_tr_b16 v[74:75], %8 offset:5120\n"
    "ds_read_b128 v[68:71], %5 offset:6144\n"
    "v_mfma_f32_32x32x16_bf16 %1, v[92:95], v[80:83], %1\n"
    "ds_read_b64_tr_b16 v[76:77], %9 offset:4096\n"
    "ds_read_b64_tr_b16 v[78:79], %9 offset:5120\n"
    "v_mfma_f32_32x32x16_bf16 %3, v[92:95], v[84:87], %3\n"
    "s_waitcnt lgkmcnt(0)\n"
    "v_mfma_f32_32x32x16_bf16 %0, v[72:75], v[64:67], %0\n"
    "ds_read_b128 v[80:83], %5 offset:3072\n"
    "ds_read_b64_tr_b16 v[88:89], %8 offset:6144\n"
    "v_mfma_f32_32x32x16_bf16 %2, v[72:75], v[68:71], %2\n"
    "ds_read_b64_tr_b16 v[90:91], %8 offset:7168\n"
    "ds_read_b128 v[84:87], %5 offset:7168\n"
    "v_mfma_f32_32x32x16_bf16 %1, v[76:79], v[64:67], %1\n"
    "ds_read_b64_tr_b16 v[92:93], %9 offset:6144\n"
    "ds_read_b64_tr_b16 v[94:95], %9 offset:7168\n"
    "v_mfma_f32_32x32x16_bf16 %3, v[76:79], v[68:71], %3\n"
    "s_waitcnt lgkmcnt(0)\n"
    "s_barrier\n"
    "v_mfma_f32_32x32x16_bf16 %0, v[88:91], v[80:83], %0\n"
    "ds_read_b128 v[64:67], %5 offset:32768\n"
    "ds_read_b64_tr_b16 v[72:73], %8 offset:32768\n"
    "v_mfma_f32_32x32x16_bf16 %2, v[88:91], v[84:87], %2\n"
    "ds_read_b64_tr_b16 v[74:75], %8 offset:33792\n"
    "ds_read_b128 v[68:71], %5 offset:36864\n"
    "v_mfma_f32_32x32x16_bf16 %1, v[92:95], v[80:83], %1\n"
    "ds_read_b64_tr_b16 v[76:77], %9 offset:32768\n"
    "ds_read_b64_tr_b16 v[78:79], %9 offset:33792\n"
    "v_mfma_f32_32x32x16_bf16 %3, v[92:95], v[84:87], %3\n"
    "s_waitcnt lgkmcnt(0)\n"
    "v_mfma_f32_32x32x16_bf16 %0, v[72:75], v[64:67], %0\n"
    "ds_read_b128 v[80:83], %5 offset:33792\n"
    "ds_read_b64_tr_b16 v[88:89], %8 offset:34816\n"
    "v_mfma_f32_32x32x16_bf16 %2, v[72:75], v[68:71], %2\n"
    "ds_read_b64_tr_b16 v[90:91], %8 offset:35840\n"
    "ds_read_b128 v[84:87], %5 offset:37888\n"
    "v_mfma_f32_32x32x16_bf16 %1, v[76:79], v[64:67], %1\n"
    "ds_read_b64_tr_b16 v[92:93], %9 offset:34816\n"
    "ds_read_b64_tr_b16 v[94:95], %9 offset:35840\n"
    "v_mfma_f32_32x32x16_bf16 %3, v[76:79], v[68:71], %3\n"
    "s_waitcnt lgkmcnt(0)\n"
    "v_mfma_f32_32x32x16_bf16 %0, v[88:91], v[80:83], %0\n"
    "ds_read_b128 v[64:67], %5 offset:34816\n"
    "ds_read_b64_tr_b16 v[72:73], %8 offset:36864\n"
    "v_mfma_f32_32x32x16_bf16 %2, v[88:91], v[84:87], %2\n"
    "ds_read_b64_tr_b16 v[74:75], %8 offset:37888\n"
    "ds_read_b128 v[68:71], %5 offset:38912\n"
    "v_mfma_f32_32x32x16_bf16 %1, v[92:95], v[80:83], %1\n"
    "ds_read_b64_tr_b16 v[76:77], %9 offset:36864\n"
    "ds_read_b64_tr_b16 v[78:79], %9 offset:37888\n"
    "v_mfma_f32_32x32x16_bf16 %3, v[92:95], v[84:87], %3\n"
    "s_waitcnt lgkmcnt(0)\n"
    "v_mfma_f32_32x32x16_bf16 %0, v[72:75], v[64:67], %0\n"
    "ds_read_b128 v[80:83], %5 offset:35840\n"
    "ds_read_b64_tr_b16 v[88:89], %8 offset:38912\n"
    "v_mfma_f32_32x32x16_bf16 %2, v[72:75], v[68:71], %2\n"
    "ds_read_b64_tr_b16 v[90:91], %8 offset:39936\n"
    "ds_read_b128 v[84:87], %5 offset:39936\n"
    "v_mfma_f32_32x32x16_bf16 %1, v[76:79], v[64:67], %1\n"
    "ds_read_b64_tr_b16 v[92:93], %9 offset:38912\n"
    "ds_read_b64_tr_b16 v[94:95], %9 offset:39936\n"
    "v_mfma_f32_32x32x16_bf16 %3, v[76:79], v[68:71], %3\n"
    "s_waitcnt lgkmcnt(0)\n"
    "s_barrier\n"
    "v_mfma_f32_32x32x16_bf16 %0, v[88:91], v[80:83], %0\n"
    "ds_read_b128 v[64:67], %4 offset:0\n"
    "ds_read_b64_tr_b16 v[72:73], %6 offset:0\n"
    "v_mfma_f32_32x32x16_bf16 %2, v[88:91], v[84:87], %2\n"
    "ds_read_b64_tr_b16 v[74:75], %6 offset:1024\n"
    "ds_read_b128 v[68:71], %4 offset:4096\n"
    "v_mfma_f32_32x32x16_bf16 %1, v[92:95], v[80:83], %1\n"
    "ds_read_b64_tr_b16 v[76:77], %7 offset:0\n"
    "ds_read_b64_tr_b16 v[78:79], %7 offset:1024\n"
    "v_mfma_f32_32x32x16_bf16 %3, v[92:95], v[84:87], %3\n"
    "s_sub_u32 s42, s42, 1\n"
    "s_cbranch_scc0 Lout_loop_%=\n"
    "s_waitcnt lgkmcnt(0)\n"
    "v_mfma_f32_32x32x16_bf16 %0, v[72:75], v[64:67], %0\n"
    "ds_read_b128 v[80:83], %4 offset:1024\n"
    "ds_read_b64_tr_b16 v[88:89], %6 offset:2048\n"
    "v_mfma_f32_32x32x16_bf16 %2, v[72:75], v[68:71], %2\n"
    "ds_read_b64_tr_b16 v[90:91], %6 offset:3072\n"
    "ds_read_b128 v[84:87], %4 offset:5120\n"
    "v_mfma_f32_32x32x16_bf16 %1, v[76:79], v[64:67], %1\n"
    "ds_read_b64_tr_b16 v[92:93], %7 offset:2048\n"
    "ds_read_b64_tr_b16 v[94:95], %7 offset:3072\n"
    "v_mfma_f32_32x32x16_bf16 %3, v[76:79], v[68:71], %3\n"
    "s_waitcnt lgkmcnt(0)\n"
    "v_mfma_f32_32x32x16_bf16 %0, v[88:91], v[80:83], %0\n"
    "ds_read_b128 v[64:67], %4 offset:2048\n"
    "ds_read_b64_tr_b16 v[72:73], %6 offset:4096\n"
    "v_mfma_f32_32x32x16_bf16 %2, v[88:91], v[84:87], %2\n"
    "ds_read_b64_tr_b16 v[74:75], %6 offset:5120\n"
    "ds_read_b128 v[68:71], %4 offset:6144\n"
    "v_mfma_f32_32x32x16_bf16 %1, v[92:95], v[80:83], %1\n"
    "ds_read_b64_tr_b16 v[76:77], %7 offset:4096\n"
    "ds_read_b64_tr_b16 v[78:79], %7 offset:5120\n"
    "v_mfma_f32_32x32x16_bf16 %3, v[92:95], v[84:87], %3\n"
    "s_waitcnt lgkmcnt(0)\n"
    "v_mfma_f32_32x32x16_bf16 %0, v[72:75], v[64:67], %0\n"
    "ds_read_b128 v[80:83], %4 offset:3072\n"
    "ds_read_b64_tr_b16 v[88:89], %6 offset:6144\n"
    "v_mfma_f32_32x32x16_bf16 %2, v[72:75], v[68:71], %2\n"
    "ds_read_b64_tr_b16 v[90:91], %6 offset:7168\n"
    "ds_read_b128 v[84:87], %4 offset:7168\n"
    "v_mfma_f32_32x32x16_bf16 %1, v[76:79], v[64:67], %1\n"
    "ds_read_b64_tr_b16 v[92:93], %7 offset:6144\n"
    "ds_read_b64_tr_b16 v[94:95], %7 offset:7168\n"
    "v_mfma_f32_32x32x16_bf16 %3, v[76:79], v[68:71], %3\n"
    "s_waitcnt lgkmcnt(0)\n"
    "s_barrier\n"
    "v_mfma_f32_32x32x16_bf16 %0, v[88:91], v[80:83], %0\n"
    "ds_read_b128 v[64:67], %4 offset:32768\n"
    "ds_read_b64_tr_b16 v[72:73], %6 offset:32768\n"
    "v_mfma_f32_32x32x16_bf16 %2, v[88:91], v[84:87], %2\n"
    "ds_read_b64_tr_b16 v[74:75], %6 offset:33792\n"
    "ds_read_b128 v[68:71], %4 offset:36864\n"
    "v_mfma_f32_32x32x16_bf16 %1, v[92:95], v[80:83], %1\n"
    "ds_read_b64_tr_b16 v[76:77], %7 offset:32768\n"
    "ds_read_b64_tr_b16 v[78:79], %7 offset:33792\n"
    "v_mfma_f32_32x32x16_bf16 %3, v[92:95], v[84:87], %3\n"
    "s_waitcnt lgkmcnt(0)\n"
    "v_mfma_f32_32x32x16_bf16 %0, v[72:75], v[64:67], %0\n"
    "ds_read_b128 v[80:83], %4 offset:33792\n"
    "ds_read_b64_tr_b16 v[88:89], %6 offset:34816\n"
    "v_mfma_f32_32x32x16_bf16 %2, v[72:75], v[68:71], %2\n"
    "ds_read_b64_tr_b16 v[90:91], %6 offset:35840\n"
    "ds_read_b128 v[84:87], %4 offset:37888\n"
    "v_mfma_f32_32x32x16_bf16 %1, v[76:79], v[64:67], %1\n"
    "ds_read_b64_tr_b16 v[92:93], %7 offset:34816\n"
    "ds_read_b64_tr_b16 v[94:95], %7 offset:35840\n"
    "v_mfma_f32_32x32x16_bf16 %3, v[76:79], v[68:71], %3\n"
    "s_waitcnt lgkmcnt(0)\n"
    "v_mfma_f32_32x32x16_bf16 %0, v[88:91], v[80:83], %0\n"
    "ds_read_b128 v[64:67], %4 offset:34816\n"
    "ds_read_b64_tr_b16 v[72:73], %6 offset:36864\n"
    "v_mfma_f32_32x32x16_bf16 %2, v[88:91], v[84:87], %2\n"
    "ds_read_b64_tr_b16 v[74:75], %6 offset:37888\n"
    "ds_read_b128 v[68:71], %4 offset:38912\n"
    "v_mfma_f32_32x32x16_bf16 %1, v[92:95], v[80:83], %1\n"
    "ds_read_b64_tr_b16 v[76:77], %7 offset:36864\n"
    "ds_read_b64_tr_b16 v[78:79], %7 offset:37888\n"
    "v_mfma_f32_32x32x16_bf16 %3, v[92:95], v[84:87], %3\n"
    "s_waitcnt lgkmcnt(0)\n"
    "v_mfma_f32_32x32x16_bf16 %0, v[72:75], v[64:67], %0\n"
    "ds_read_b128 v[80:83], %4 offset:35840\n"
    "ds_read_b64_tr_b16 v[88:89], %6 offset:38912\n"
    "v_mfma_f32_32x32x16_bf16 %2, v[72:75], v[68:71], %2\n"
    "ds_read_b64_tr_b16 v[90:91], %6 offset:39936\n"
    "ds_read_b128 v[84:87], %4 offset:39936\n"
    "v_mfma_f32_32x32x16_bf16 %1, v[76:79], v[64:67], %1\n"
    "ds_read_b64_tr_b16 v[92:93], %7 offset:38912\n"
    "ds_read_b64_tr_b16 v[94:95], %7 offset:39936\n"
    "v_mfma_f32_32x32x16_bf16 %3, v[76:79], v[68:71], %3\n"
    "s_waitcnt lgkmcnt(0)\n"
    "s_barrier\n"
    "v_mfma_f32_32x32x16_bf16 %0, v[88:91], v[80:83], %0\n"
    "ds_read_b128 v[64:67], %5 offset:0\n"
    "ds_read_b64_tr_b16 v[72:73], %8 offset:0\n"
    "v_mfma_f32_32x32x16_bf16 %2, v[88:91], v[84:87], %2\n"
    "ds_read_b64_tr_b16 v[74:75], %8 offset:1024\n"
    "ds_read_b128 v[68:71], %5 offset:4096\n"
    "v_mfma_f32_32x32x16_bf16 %1, v[92:95], v[80:83], %1\n"
    "ds_read_b64_tr_b16 v[76:77], %9 offset:0\n"
    "ds_read_b64_tr_b16 v[78:79], %9 offset:1024\n"
    "v_mfma_f32_32x32x16_bf16 %3, v[92:95], v[84:87], %3\n"
    "s_waitcnt lgkmcnt(0)\n"
    "v_mfma_f32_32x32x16_bf16 %0, v[72:75], v[64:67], %0\n"
    "ds_read_b128 v[80:83], %5 offset:1024\n"
    "ds_read_b64_tr_b16 v[88:89], %8 offset:2048\n"
    "v_mfma_f32_32x32x16_bf16 %2, v[72:75], v[68:71], %2\n"
    "ds_read_b64_tr_b16 v[90:91], %8 offset:3072\n"
    "ds_read_b128 v[84:87], %5 offset:5120\n"
    "v_mfma_f32_32x32x16_bf16 %1, v[76:79], v[64:67], %1\n"
    "ds_read_b64_tr_b16 v[92:93], %9 offset:2048\n"
    "ds_read_b64_tr_b16 v[94:95], %9 offset:3072\n"
    "v_mfma_f32_32x32x16_bf16 %3, v[76:79], v[68:71], %3\n"
    "s_waitcnt lgkmcnt(0)\n"
    "v_mfma_f32_32x32x16_bf16 %0, v[88:91], v[80:83], %0\n"
    "ds_read_b128 v[64:67], %5 offset:2048\n"
    "ds_read_b64_tr_b16 v[72:73], %8 offset:4096\n"
    "v_mfma_f32_32x32x16_bf16 %2, v[88:91], v[84:87], %2\n"
    "ds_read_b64_tr_b16 v[74:75], %8 offset:5120\n"
    "ds_read_b128 v[68:71], %5 offset:6144\n"
    "v_mfma_f32_32x32x16_bf16 %1, v[92:95], v[80:83], %1\n"
    "ds_read_b64_tr_b16 v[76:77], %9 offset:4096\n"
    "ds_read_b64_tr_b16 v[78:79], %9 offset:5120\n"
    "v_mfma_f32_32x32x16_bf16 %3, v[92:95], v[84:87], %3\n"
    "s_waitcnt lgkmcnt(0)\n"
    "v_mfma_f32_32x32x16_bf16 %0, v[72:75], v[64:67], %0\n"
    "ds_read_b128 v[80:83], %5 offset:3072\n"
    "ds_read_b64_tr_b16 v[88:89], %8 offset:6144\n"
    "v_mfma_f32_32x32x16_bf16 %2, v[72:75], v[68:71], %2\n"
    "ds_read_b64_tr_b16 v[90:91], %8 offset:7168\n"
    "ds_read_b128 v[84:87], %5 offset:7168\n"
    "v_mfma_f32_32x32x16_bf16 %1, v[76:79], v[64:67], %1\n"
    "ds_read_b64_tr_b16 v[92:93], %9 offset:6144\n"
    "ds_read_b64_tr_b16 v[94:95], %9 offset:7168\n"
    "v_mfma_f32_32x32x16_bf16 %3, v[76:79], v[68:71], %3\n"
    "s_waitcnt lgkmcnt(0)\n"
    "s_barrier\n"
    "v_mfma_f32_32x32x16_bf16 %0, v[88:91], v[80:83], %0\n"
    "ds_read_b128 v[64:67], %5 offset:32768\n"
    "ds_read_b64_tr_b16 v[72:73], %8 offset:32768\n"
    "v_mfma_f32_32x32x16_bf16 %2, v[88:91], v[84:87], %2\n"
    "ds_read_b64_tr_b16 v[74:75], %8 offset:33792\n"
    "ds_read_b128 v[68:71], %5 offset:36864\n"
    "v_mfma_f32_32x32x16_bf16 %1, v[92:95], v[80:83], %1\n"
    "ds_read_b64_tr_b16 v[76:77], %9 offset:32768\n"
    "ds_read_b64_tr_b16 v[78:79], %9 offset:33792\n"
    "v_mfma_f32_32x32x16_bf16 %3, v[92:95], v[84:87], %3\n"
    "s_waitcnt lgkmcnt(0)\n"
    "v_mfma_f32_32x32x16_bf16 %0, v[72:75], v[64:67], %0\n"
    "ds_read_b128 v[80:83], %5 offset:33792\n"
    "ds_read_b64_tr_b16 v[88:89], %8 offset:34816\n"
    "v_mfma_f32_32x32x16_bf16 %2, v[72:75], v[68:71], %2\n"
    "ds_read_b64_tr_b16 v[90:91], %8 offset:35840\n"
    "ds_read_b128 v[84:87], %5 offset:37888\n"
    "v_mfma_f32_32x32x16_bf16 %1, v[76:79], v[64:67], %1\n"
    "ds_read_b64_tr_b16 v[92:93], %9 offset:34816\n"
    "ds_read_b64_tr_b16 v[94:95], %9 offset:35840\n"
    "v_mfma_f32_32x32x16_bf16 %3, v[76:79], v[68:71], %3\n"
    "s_waitcnt lgkmcnt(0)\n"
    "v_mfma_f32_32x32x16_bf16 %0, v[88:91], v[80:83], %0\n"
    "ds_read_b128 v[64:67], %5 offset:34816\n"
    "ds_read_b64_tr_b16 v[72:73], %8 offset:36864\n"
    "v_mfma_f32_32x32x16_bf16 %2, v[88:91], v[84:87], %2\n"
    "ds_read_b64_tr_b16 v[74:75], %8 offset:37888\n"
    "ds_read_b128 v[68:71], %5 offset:38912\n"
    "v_mfma_f32_32x32x16_bf16 %1, v[92:95], v[80:83], %1\n"
    "ds_read_b64_tr_b16 v[76:77], %9 offset:36864\n"
    "ds_read_b64_tr_b16 v[78:79], %9 offset:37888\n"
    "v_mfma_f32_32x32x16_bf16 %3, v[92:95], v[84:87], %3\n"
    "s_waitcnt lgkmcnt(0)\n"
    "v_mfma_f32_32x32x16_bf16 %0, v[72:75], v[64:67], %0\n"
    "ds_read_b128 v[80:83], %5 offset:35840\n"
    "ds_read_b64_tr_b16 v[88:89], %8 offset:38912\n"
    "v_mfma_f32_32x32x16_bf16 %2, v[72:75], v[68:71], %2\n"
    "ds_read_b64_tr_b16 v[90:91], %8 offset:39936\n"
    "ds_read_b128 v[84:87], %5 offset:39936\n"
    "v_mfma_f32_32x32x16_bf16 %1, v[76:79], v[64:67], %1\n"
    "ds_read_b64_tr_b16 v[92:93], %9 offset:38912\n"
    "ds_read_b64_tr_b16 v[94:95], %9 offset:39936\n"
    "v_mfma_f32_32x32x16_bf16 %3, v[76:79], v[68:71], %3\n"
    "s_waitcnt lgkmcnt(0)\n"
    "v_mfma_f32_32x32x16_bf16 %0, v[88:91], v[80:83], %0\n"
    "v_mfma_f32_32x32x16_bf16 %2, v[88:91], v[84:87], %2\n"
    "v_mfma_f32_32x32x16_bf16 %1, v[92:95], v[80:83], %1\n"
    "v_mfma_f32_32x32x16_bf16 %3, v[92:95], v[84:87], %3\n"
    "s_nop 15\n"
    "Ldone_%=:\n"
        : "+v"(c00), "+v"(c01), "+v"(c10), "+v"(c11)
        : "v"(ooff0), "v"(ooff0 + 65536), "v"(woff[0]), "v"(woff[1]), "v"(woff[0] + 65536), "v"(woff[1] + 65536), "v"(wsv[0]), "v"(wsv[1]), "v"(wsv[2]), "v"(wsv[3]), "v"(os),
          "s"(orr), "s"(wor), "s"(lo), "s"(lw), "v"(fo), "v"(opoll), "v"(tag), "s"(wid)
        : "v64", "v65", "v66", "v67", "v68", "v69", "v70", "v71", "v72", "v73", "v74", "v75", "v76", "v77", "v78", "v79", "v80", "v81", "v82", "v83", "v84", "v85", "v86", "v87", "v88", "v89", "v90", "v91", "v92", "v93", "v94", "v95", "v96", "s40", "s41", "s42", "s43", "s44", "s45", "s46", "s47", "s48", "s49", "s50", "s51", "s52", "s53", "s54", "s55", "s56", "s57", "vcc", "scc", "memory");
    if (wid < 4) {
      const int lz = lane_id(), hi = lz >> 5, l31 = lz & 31;
      const __amdgpu_buffer_rsrc_t outr = __builtin_amdgcn_make_buffer_rsrc((void*)out, (short)0, 2048 * 4096, 0x00020000);
      const unsigned doff = (unsigned)(rb2 * 128 + tm * 64 + l31) * 4096u + (unsigned)(b * 1024 + cb * 128 + cn * 64 + 8 * hi) * 2u;
      __builtin_amdgcn_raw_buffer_store_b128(pack8(c00, 0), outr, doff, 0, OUT_AUX); __builtin_amdgcn_raw_buffer_store_b128(pack8(c00, 1), outr, doff + 32, 0, OUT_AUX);
      __builtin_amdgcn_raw_buffer_store_b128(pack8(c01, 0), outr, doff + 64, 0, OUT_AUX); __builtin_amdgcn_raw_buffer_store_b128(pack8(c01, 1), outr, doff + 96, 0, OUT_AUX);
      __builtin_amdgcn_raw_buffer_store_b128(pack8(c10, 0), outr, doff + 32 * 4096, 0, OUT_AUX); __builtin_amdgcn_raw_buffer_store_b128(pack8(c10, 1), outr, doff + 32 * 4096 + 32, 0, OUT_AUX);
      __builtin_amdgcn_raw_buffer_store_b128(pack8(c11, 0), outr, doff + 32 * 4096 + 64, 0, OUT_AUX); __builtin_amdgcn_raw_buffer_store_b128(pack8(c11, 1), outr, doff + 32 * 4096 + 96, 0, OUT_AUX);
    }
  }
  if (hp == 0 && rb == 0 && tid == 0) __hip_atomic_store(ctl, tag, __ATOMIC_RELAXED, __HIP_MEMORY_SCOPE_AGENT);
}

extern "C" void kernel_launch(void* const* d_in, const int* in_sizes, int n_in, void* d_out, int out_size, void* d_ws, size_t ws_size, hipStream_t stream) {
  mha_fused<<<256, 512, 0, stream>>>((const uint16_t*)d_in[0], (const uint16_t*)d_in[1], (const uint16_t*)d_in[2],
                                     (const uint16_t*)d_in[3], (const uint16_t*)d_in[4], (const uint16_t*)d_in[5], (char*)d_ws, (uint16_t*)d_out);
}
```
